```python
import torch

def _softmax(x, axis=-1):
    return torch.softmax(x, dim=axis)
def _gelu(x):
    return torch.nn.functional.gelu(x, approximate="tanh")
def _silu(x):
    return torch.nn.functional.silu(x)
def _sigmoid(x):
    return torch.sigmoid(x)
def _layernorm(x, axis=-1, eps=1e-5):
    m = x.mean(dim=axis, keepdim=True)
    v = x.var(dim=axis, keepdim=True, unbiased=False)
    return (x - m) / torch.sqrt(v + eps)
def _scatter_add(tgt, idx, src, axis=0):
    return tgt.clone().index_add_(axis, idx, src)
def _segment_sum(src, seg, n):
    out = torch.zeros((n,) + src.shape[1:], dtype=src.dtype)
    return out.index_add_(0, seg, src)
def _pad(x, pw):
    flat = tuple(v for pair in reversed(pw) for v in pair)
    return torch.nn.functional.pad(x, flat)

kernel_name = 'parallel_sum__linear_attn_block__mixer_token_mlp__prompt'

def setup_inputs():

    _w = (lambda a: (lambda t: t.to(torch.bfloat16) if t.is_floating_point() else t)(torch.as_tensor(a)))
    def _A_setup():
        B, S, D = 1, 2048, 576
        return {
            "x": _w((lambda *s: torch.randn(*s, dtype=torch.bfloat16) * 0.05)(B, S, D)),
            "wqkv": _w((lambda *s: torch.randn(*s, dtype=torch.bfloat16) * 0.05)(D, 3 * D)),
            "wo": _w((lambda *s: torch.randn(*s, dtype=torch.bfloat16) * 0.05)(D, D)),
        }
    _A_in = _A_setup()

    def _B_setup():
        B, S, D, F = 1, 2048, 576, 4608
        return {
            "x": _w((lambda *s: torch.randn(*s, dtype=torch.bfloat16) * 0.05)(B, S, D)),
            "w1": _w((lambda *s: torch.randn(*s, dtype=torch.bfloat16) * 0.4)(S, F) * (S ** -0.5)),
            "b1": _w((lambda *s: torch.randn(*s, dtype=torch.bfloat16) * 3.2)(F) * 0.02),
            "w2": _w((lambda *s: torch.randn(*s, dtype=torch.bfloat16) * 0.2)(F, S) * (F ** -0.5)),
            "b2": _w((lambda *s: torch.randn(*s, dtype=torch.bfloat16) * 0.4)(S) * 0.02),
        }
    _B_in = _B_setup()

    return {
        "A_x": _A_in["x"],
        "A_wqkv": _A_in["wqkv"],
        "A_wo": _A_in["wo"],
        "B_x": _B_in["x"],
        "B_w1": _B_in["w1"],
        "B_b1": _B_in["b1"],
        "B_w2": _B_in["w2"],
        "B_b2": _B_in["b2"],
    }

def reference(A_x, A_wqkv, A_wo, B_x, B_w1, B_b1, B_w2, B_b2):
    def _A_ref(x, wqkv, wo):
        B, S, D = x.shape
        H = 32; Dh = D // H
        qkv = (lambda x, *d: x.permute(*d))((x @ wqkv).reshape(B, S, 3, H, Dh), 2, 0, 3, 1, 4)
        q, k, v = qkv[0], qkv[1], qkv[2]
        phi = lambda t: torch.where(t > 0, t + 1, torch.exp(t)) + 1e-6
        pq, pk = phi(q), phi(k)
        kv = torch.einsum("bhsd,bhse->bhde", pk, v)
        z = torch.einsum("bhsd->bhd", pk)
        num = torch.einsum("bhqd,bhde->bhqe", pq, kv)
        den = torch.einsum("bhqd,bhd->bhq", pq, z)[..., None] + 1e-6
        o = num / den
        return (lambda x, *d: x.permute(*d))(o, 0, 2, 1, 3).reshape(B, S, D) @ wo

    def _B_ref(x, w1, b1, w2, b2):
        B, S, D = x.shape
        h = _layernorm(x, axis=-1)
        u = torch.einsum("bsd,sf->bdf", h, w1) + b1
        u = _gelu(u)
        y = torch.einsum("bdf,fs->bds", u, w2) + b2
        return (lambda x, *d: x.permute(*d))(y, 0, 2, 1)

    x = A_x
    xn = _layernorm(x, axis=-1)
    return x + _A_ref(xn, A_wqkv, A_wo) + _B_ref(xn, B_w1, B_b1, B_w2, B_b2)
```

```cpp
#define PROBE 4
#include <hip/hip_runtime.h>
#include <hip/hip_bf16.h>
typedef unsigned short u16;
typedef __bf16 bf16x8 __attribute__((ext_vector_type(8)));
typedef __bf16 bf16x4 __attribute__((ext_vector_type(4)));
typedef float f32x4 __attribute__((ext_vector_type(4)));
typedef unsigned u32x4 __attribute__((ext_vector_type(4)));
#define LAS __attribute__((address_space(3)))
constexpr int S = 2048, D = 576, F = 4608, H = 32, DH = 18;

__device__ __forceinline__ float bf2f(u16 v) { return __uint_as_float((unsigned)v << 16); }
__device__ __forceinline__ u16 f2bf(float f) { __bf16 b = (__bf16)f; return __builtin_bit_cast(u16, b); }
__device__ __forceinline__ float rbf(float f) { return bf2f(f2bf(f)); }
__device__ __forceinline__ float wave_sum(float v) {
#pragma unroll
  for (int o = 32; o > 0; o >>= 1) v += __shfl_xor(v, o);
  return v;
}
__device__ __forceinline__ void ln9(float (&v)[9]) {
  float s = 0;
#pragma unroll
  for (int j = 0; j < 9; ++j) s += v[j];
  s = wave_sum(s);
  const float mean = s * (1.0f / D);
  float q = 0;
#pragma unroll
  for (int j = 0; j < 9; ++j) { float d = v[j] - mean; q += d * d; }
  q = wave_sum(q);
  const float mb = rbf(mean), vb = rbf(q * (1.0f / D));
  const float den = rbf(sqrtf(rbf(vb + 1e-5f)));
#pragma unroll
  for (int j = 0; j < 9; ++j) v[j] = rbf(rbf(v[j] - mb) / den);
}
__global__ __launch_bounds__(256) void k_ln(const u16* x, u16* xn, u16* hT) {
  const int w = threadIdx.x >> 6, lane = threadIdx.x & 63, row = blockIdx.x * 4 + w;
  float v[9];
#pragma unroll
  for (int j = 0; j < 9; ++j) v[j] = bf2f(x[row * D + j * 64 + lane]);
  ln9(v);
#pragma unroll
  for (int j = 0; j < 9; ++j) xn[row * D + j * 64 + lane] = f2bf(v[j]);
  ln9(v);
#pragma unroll
  for (int j = 0; j < 9; ++j) hT[(j * 64 + lane) * S + row] = f2bf(v[j]);
}
__device__ __forceinline__ float gelu_tanh(float u) {
  const float c = 0.7978845608028654f;
  float t = c * (u + 0.044715f * u * u * u);
  float e = __expf(2.0f * t);
  float th = 1.0f - 2.0f / (e + 1.0f);
  return 0.5f * u * (1.0f + th);
}
template <int EPI>
__global__ __launch_bounds__(256) void k_gemm(const u16* A, int lda, const u16* B, int ldb, int K, u16* C, int ldc, const u16* bias, const u16* x, const u16* attn) {
  __shared__ __attribute__((aligned(16))) u16 As[64 * 40];
  __shared__ __attribute__((aligned(16))) u16 Bs[32 * 72];
  const int tid = threadIdx.x, lane = tid & 63, w = tid >> 6, wm = w >> 1, wn = w & 1;
  const int m0 = blockIdx.y * 64, n0 = blockIdx.x * 64;
  f32x4 acc[2][2];
#pragma unroll
  for (int i = 0; i < 2; ++i)
#pragma unroll
    for (int j = 0; j < 2; ++j) acc[i][j] = (f32x4){0, 0, 0, 0};
  const u16* ga = A + (size_t)(m0 + tid / 4) * lda + (tid % 4) * 8;
  const u16* gb = B + (size_t)(tid / 8) * ldb + n0 + (tid % 8) * 8;
  u32x4 ra = *(const u32x4*)ga, rb = *(const u32x4*)gb;
  for (int k0 = 0; k0 < K; k0 += 32) {
    __syncthreads();
    *(u32x4*)(As + (tid / 4) * 40 + (tid % 4) * 8) = ra;
    *(u32x4*)(Bs + (tid / 8) * 72 + (tid % 8) * 8) = rb;
    __syncthreads();
    if (k0 + 32 < K) { ra = *(const u32x4*)(ga + k0 + 32); rb = *(const u32x4*)(gb + (size_t)(k0 + 32) * ldb); }
    bf16x8 a[2], b[2];
#pragma unroll
    for (int i = 0; i < 2; ++i) a[i] = *(const bf16x8*)(As + (wm * 32 + i * 16 + (lane & 15)) * 40 + (lane >> 4) * 8);
#pragma unroll
    for (int j = 0; j < 2; ++j) {
      const int g = lane >> 4, q = (lane & 15) >> 2, p = lane & 3;
      const u16* bp = Bs + (8 * g + q) * 72 + wn * 32 + j * 16 + 4 * p;
      bf16x4 lo = __builtin_amdgcn_ds_read_tr16_b64_v4bf16((LAS bf16x4*)bp);
      bf16x4 hi = __builtin_amdgcn_ds_read_tr16_b64_v4bf16((LAS bf16x4*)(bp + 4 * 72));
      b[j] = __builtin_shufflevector(lo, hi, 0, 1, 2, 3, 4, 5, 6, 7);
    }
#pragma unroll
    for (int i = 0; i < 2; ++i)
#pragma unroll
      for (int j = 0; j < 2; ++j) acc[i][j] = __builtin_amdgcn_mfma_f32_16x16x32_bf16(a[i], b[j], acc[i][j], 0, 0, 0);
  }
#pragma unroll
  for (int i = 0; i < 2; ++i)
#pragma unroll
    for (int j = 0; j < 2; ++j)
#pragma unroll
      for (int r = 0; r < 4; ++r) {
        const int m = m0 + wm * 32 + i * 16 + (lane >> 4) * 4 + r, n = n0 + wn * 32 + j * 16 + (lane & 15);
        float v = acc[i][j][r];
        if (EPI == 0) C[(size_t)m * ldc + n] = f2bf(v);
        if (EPI == 1) C[(size_t)m * ldc + n] = f2bf(gelu_tanh(rbf(rbf(v) + bf2f(bias[n]))));
        if (EPI == 2) { float y = rbf(rbf(v) + bf2f(bias[n])); float xa = rbf(bf2f(x[n * D + m]) + bf2f(attn[n * D + m])); C[n * D + m] = f2bf(xa + y); }
      }
}
__device__ __forceinline__ float phi(float t) { return rbf(rbf(t > 0 ? t + 1.0f : __expf(t)) + 1e-6f); }
__global__ __launch_bounds__(384) void k_kv(const u16* qkv, float* part) {
  __shared__ float sk[256 * 18], sv[256 * 18];
  const int h = blockIdx.x, c = blockIdx.y, tid = threadIdx.x;
  for (int i = tid; i < 256 * 18; i += 384) { int r = i / 18, e = i % 18; const u16* row = qkv + (size_t)(c * 256 + r) * 1728; sk[i] = phi(bf2f(row[576 + h * 18 + e])); sv[i] = bf2f(row[1152 + h * 18 + e]); }
  __syncthreads();
  if (tid < 342) {
    float s = 0;
    if (tid < 324) { int i = tid / 18, j = tid % 18; for (int r = 0; r < 256; ++r) s += sk[r * 18 + i] * sv[r * 18 + j]; }
    else { int i = tid - 324; for (int r = 0; r < 256; ++r) s += sk[r * 18 + i]; }
    part[(c * 32 + h) * 342 + tid] = s;
  }
}
__global__ __launch_bounds__(256) void k_o(const u16* qkv, const float* part, u16* o) {
  __shared__ float kv[32 * 342];
  for (int i = threadIdx.x; i < 32 * 342; i += 256) { float s = 0; for (int c = 0; c < 8; ++c) s += part[c * 32 * 342 + i]; kv[i] = rbf(s); }
  __syncthreads();
  const int r = blockIdx.x * 8 + (threadIdx.x >> 5), h = threadIdx.x & 31;
  float pq[18];
#pragma unroll
  for (int e = 0; e < 18; ++e) pq[e] = phi(bf2f(qkv[(size_t)r * 1728 + h * 18 + e]));
  float den = 0;
#pragma unroll
  for (int e = 0; e < 18; ++e) den += pq[e] * kv[h * 342 + 324 + e];
  den = rbf(rbf(den) + 1e-6f);
#pragma unroll
  for (int j = 0; j < 18; ++j) {
    float num = 0;
#pragma unroll
    for (int e = 0; e < 18; ++e) num += pq[e] * kv[h * 342 + e * 18 + j];
    o[(size_t)r * D + h * 18 + j] = f2bf(rbf(num) / den);
  }
}
__global__ __launch_bounds__(256) void probe(const char* A, unsigned region, unsigned* sink) {
  const int tid = threadIdx.x;
  const u32x4* p = (const u32x4*)(A + (size_t)blockIdx.x * region);
  unsigned s = 0; int n = region / 16;
  for (int i = tid; i < n; i += 256 * 8) {
    u32x4 v[8];
#pragma unroll
    for (int j = 0; j < 8; ++j) v[j] = p[i + j * 256];
#pragma unroll
    for (int j = 0; j < 8; ++j) s += v[j][0] ^ v[j][3];
  }
  if (s == 0x12345678) *sink = s;
}
template<int NT>
__global__ __launch_bounds__(256) void rd_all(const u32x4* a, size_t n, const u32x4* b, size_t m, unsigned* sink) {
  unsigned s = 0; size_t st = (size_t)gridDim.x * 256;
  for (size_t i = blockIdx.x * 256 + threadIdx.x; i < n; i += st * 4) {
    u32x4 v[4];
#pragma unroll
    for (int j = 0; j < 4; ++j) { size_t k = i + j * st; if (k >= n) k = i; v[j] = NT ? __builtin_nontemporal_load(a + k) : a[k]; }
#pragma unroll
    for (int j = 0; j < 4; ++j) s += v[j][0] ^ v[j][3];
  }
  for (size_t i = blockIdx.x * 256 + threadIdx.x; i < m; i += st * 4) {
    u32x4 v[4];
#pragma unroll
    for (int j = 0; j < 4; ++j) { size_t k = i + j * st; if (k >= m) k = i; v[j] = NT ? __builtin_nontemporal_load(b + k) : b[k]; }
#pragma unroll
    for (int j = 0; j < 4; ++j) s += v[j][0] ^ v[j][3];
  }
  if (s == 0x12345678) *sink = s;
}
extern "C" void kernel_launch(void* const* d_in, const int* in_sizes, int n_in, void* d_out, int out_size, void* d_ws, size_t ws_size, hipStream_t stream) {
  const u16* x = (const u16*)d_in[0]; const u16* wqkv = (const u16*)d_in[1]; const u16* wo = (const u16*)d_in[2];
  const u16* w1 = (const u16*)d_in[4]; const u16* b1 = (const u16*)d_in[5]; const u16* w2 = (const u16*)d_in[6]; const u16* b2 = (const u16*)d_in[7];
  u16* out = (u16*)d_out;
  char* ws = (char*)d_ws;
  u16* xn = (u16*)(ws); u16* hT = (u16*)(ws + (4 << 20)); u16* qkv = (u16*)(ws + (8 << 20)); u16* U = (u16*)(ws + (16 << 20));
  u16* o = (u16*)(ws + (24 << 20)); u16* attn = (u16*)(ws + (28 << 20)); float* part = (float*)(ws + (32 << 20));
  unsigned* sink = (unsigned*)(ws + (200u << 20));
  rd_all<0><<<1024, 256, 0, stream>>>((const u32x4*)w1, (size_t)S * F / 8, (const u32x4*)w2, (size_t)S * F / 8, sink);
  k_ln<<<S / 4, 256, 0, stream>>>(x, xn, hT);
  k_gemm<0><<<dim3(1728 / 64, S / 64), 256, 0, stream>>>(xn, D, wqkv, 1728, D, qkv, 1728, nullptr, nullptr, nullptr);
  k_kv<<<dim3(32, 8), 384, 0, stream>>>(qkv, part);
  k_o<<<S / 8, 256, 0, stream>>>(qkv, part, o);
  k_gemm<0><<<dim3(D / 64, S / 64), 256, 0, stream>>>(o, D, wo, D, D, attn, D, nullptr, nullptr, nullptr);
  k_gemm<1><<<dim3(F / 64, D / 64), 256, 0, stream>>>(hT, S, w1, F, S, U, F, b1, nullptr, nullptr);
  k_gemm<2><<<dim3(S / 64, D / 64), 256, 0, stream>>>(U, F, w2, S, F, out, D, b2, x, attn);
}
```

```cpp
#include <hip/hip_runtime.h>
#include <hip/hip_bf16.h>

typedef unsigned short u16;
typedef __bf16 bf16x8 __attribute__((ext_vector_type(8)));
typedef __bf16 bf16x4 __attribute__((ext_vector_type(4)));
typedef float f32x4 __attribute__((ext_vector_type(4)));
typedef float f32x2 __attribute__((ext_vector_type(2)));
typedef unsigned u32x4 __attribute__((ext_vector_type(4)));
typedef unsigned u32x2 __attribute__((ext_vector_type(2)));
#define LAS __attribute__((address_space(3)))
#define GAS __attribute__((address_space(1)))
typedef __amdgpu_buffer_rsrc_t rsrc_t;

constexpr int S_ = 2048, D_ = 576, F_ = 4608;
constexpr int NS = 4;
constexpr unsigned SLOT = 34816;
constexpr unsigned LDS_BYTES = 163840;
constexpr unsigned XTRA = NS * SLOT;

constexpr size_t WS_H = 0;
constexpr size_t WS_X = WS_H + 2359296;
constexpr size_t WS_U = WS_X + 2359296;
constexpr size_t WS_PQ = WS_U + 5308416;
constexpr size_t WS_KVP = WS_PQ + 2621440;
constexpr size_t WS_KVZ = WS_KVP + 700416;
constexpr size_t WS_CNT = WS_KVZ + 139264;
constexpr size_t WS_XCH = WS_CNT + 260 * 256;
constexpr int NCNT = 260;

__device__ __forceinline__ float bf2f(u16 v) { return __uint_as_float((unsigned)v << 16); }
__device__ __forceinline__ float rbf(float f) { return (float)(__bf16)f; }
__device__ __forceinline__ unsigned pack2(float lo, float hi) {
  typedef __bf16 bf16x2 __attribute__((ext_vector_type(2)));
  bf16x2 v = {(__bf16)lo, (__bf16)hi};
  return __builtin_bit_cast(unsigned, v);
}
__device__ __forceinline__ bf16x8 pack8(const f32x4& a, const f32x4& b) {
  u32x4 w = {pack2(a[0], a[1]), pack2(a[2], a[3]), pack2(b[0], b[1]), pack2(b[2], b[3])};
  return __builtin_bit_cast(bf16x8, w);
}
__device__ __forceinline__ rsrc_t mkrsrc(const void* p, unsigned bytes) { return __builtin_amdgcn_make_buffer_rsrc((void*)p, (short)0, (int)bytes, 0x00020000); }
__device__ __forceinline__ bf16x4 trread(const LAS char* p) { return __builtin_amdgcn_ds_read_tr16_b64_v4bf16((LAS bf16x4*)p); }
__device__ __forceinline__ bf16x8 cat8(bf16x4 lo, bf16x4 hi) { return __builtin_shufflevector(lo, hi, 0, 1, 2, 3, 4, 5, 6, 7); }
#define MFMA(a, b, c) __builtin_amdgcn_mfma_f32_16x16x32_bf16((a), (b), (c), 0, 0, 0)
#define FENCE() asm volatile("" ::: "memory")
#define BAR() do { FENCE(); __builtin_amdgcn_s_barrier(); FENCE(); } while (0)
#define WAIT_LGKM0() asm volatile("s_waitcnt lgkmcnt(0)" ::: "memory")
#define WAIT_VM(n) asm volatile("s_waitcnt vmcnt(%0)" ::"n"(n) : "memory")
__device__ __forceinline__ void dma(unsigned ldsaddr, unsigned voff, rsrc_t r, unsigned soff) {
  asm volatile("s_mov_b32 m0, %0\n\ts_nop 0\n\tbuffer_load_dwordx4 %1, %2, %3 offen lds" ::"s"(ldsaddr), "v"(voff), "s"(r), "s"(soff) : "memory");
}
__device__ __forceinline__ float phi(float t) { t = rbf(t); return rbf(rbf(t > 0.f ? t + 1.0f : __expf(t)) + 1e-6f); }
__device__ __forceinline__ float gelu_tanh(float u) {
  const float k0 = -2.0f * 0.7978845608028654f * 1.4426950408889634f, k1 = k0 * 0.044715f;
  const float z = u * (k0 + k1 * u * u);
  return u * __builtin_amdgcn_rcpf(1.0f + __builtin_amdgcn_exp2f(z));
}

__device__ __forceinline__ float half_sum(float v) {
#pragma unroll
  for (int o = 16; o > 0; o >>= 1) v += __shfl_xor(v, o);
  return v;
}
__device__ __forceinline__ void ln18(float (&v)[18]) {
  float s = 0.f;
#pragma unroll
  for (int j = 0; j < 18; ++j) s += v[j];
  const float mean = half_sum(s) * (1.0f / D_);
  float q = 0.f;
#pragma unroll
  for (int j = 0; j < 18; ++j) { const float d = v[j] - mean; q += d * d; }
  q = half_sum(q);
  const float mb = rbf(mean), vb = rbf(q * (1.0f / D_));
  const float den = rbf(sqrtf(rbf(vb + 1e-5f)));
#pragma unroll
  for (int j = 0; j < 18; ++j) v[j] = rbf(rbf(v[j] - mb) / den);
}
__global__ __launch_bounds__(256) void k0_ln(const u16* __restrict__ x, char* __restrict__ ws) {
  constexpr int RS = 1168;
  __shared__ __attribute__((aligned(16))) char sm[2 * 8 * RS];
  const int tid = threadIdx.x, lane = tid & 63, w = tid >> 6, b = blockIdx.x;
  if (b == 0) for (int i = tid; i < NCNT; i += 256) *(unsigned*)(ws + WS_CNT + (size_t)i * 256) = 0u;
  const int ksi = b >> 2, gw = b & 3;
  const int r = 2 * w + (lane >> 5), l32 = lane & 31;
  const int tok = 32 * ksi + (r < 4 ? 4 * gw + r : 16 + 4 * gw + r - 4);
  const unsigned* xr = (const unsigned*)(x + (size_t)tok * D_);
  float v[18];
#pragma unroll
  for (int i = 0; i < 9; ++i) { const unsigned u = xr[l32 + 32 * i]; v[2 * i] = __uint_as_float(u << 16); v[2 * i + 1] = __uint_as_float(u & 0xffff0000u); }
  ln18(v);
#pragma unroll
  for (int i = 0; i < 9; ++i) *(unsigned*)(sm + r * RS + (l32 + 32 * i) * 4) = pack2(v[2 * i], v[2 * i + 1]);
  ln18(v);
#pragma unroll
  for (int i = 0; i < 9; ++i) *(unsigned*)(sm + 8 * RS + r * RS + (l32 + 32 * i) * 4) = pack2(v[2 * i], v[2 * i + 1]);
  __syncthreads();
  {
    const LAS char* hs = (const LAS char*)sm + 8 * RS;
    const int q = (lane & 15) >> 2, p = lane & 3;
    for (int dt0 = 4 * w; dt0 < 36; dt0 += 16) {
      const int dt = dt0 + (lane >> 4);
      const bf16x4 lo = trread(hs + q * RS + (16 * dt + 4 * p) * 2), hi = trread(hs + (4 + q) * RS + (16 * dt + 4 * p) * 2);
      *(bf16x8*)(ws + WS_H + ((size_t)(ksi * 36 + dt) * 64 + gw * 16 + (lane & 15)) * 16) = cat8(lo, hi);
    }
  }
  for (int i = tid; i < 576; i += 256) {
    const int rr = i & 7, ch = i >> 3;
    const int kst = ch >> 2, g = ch & 3;
    const int t = 32 * ksi + (rr < 4 ? 4 * gw + rr : 16 + 4 * gw + rr - 4);
    const u32x2 lo = *(const u32x2*)(sm + rr * RS + (32 * kst + 4 * g) * 2), hi = *(const u32x2*)(sm + rr * RS + (32 * kst + 16 + 4 * g) * 2);
    const int rb = t >> 7, st = (t >> 4) & 7, c = t & 15;
    *(u32x4*)(ws + WS_X + ((size_t)((rb * 18 + kst) * 8 + st) * 64 + g * 16 + c) * 16) = (u32x4){lo[0], lo[1], hi[0], hi[1]};
  }
}

struct FragG { bf16x8 a[9]; bf16x8 w[2]; };
__device__ __forceinline__ void ldG(FragG& f, const LAS char* pa, const LAS char* pw0, const LAS char* pw1, int ks) {
#pragma unroll
  for (int n = 0; n < 2; ++n) { const LAS char* p = (n ? pw1 : pw0) + ks * 8192; f.w[n] = cat8(trread(p), trread(p + 4096)); }
#pragma unroll
  for (int a = 0; a < 9; ++a) f.a[a] = *(const LAS bf16x8*)(pa + ks * 9216 + a * 1024);
}
template <bool WROW>
__device__ __forceinline__ void mmaG(f32x4 (&acc)[2][9], const FragG& f) {
#pragma unroll
  for (int a = 0; a < 9; ++a)
#pragma unroll
    for (int n = 0; n < 2; ++n) acc[n][a] = WROW ? MFMA(f.w[n], f.a[a], acc[n][a]) : MFMA(f.a[a], f.w[n], acc[n][a]);
}
__device__ __forceinline__ void schedG() {
#pragma unroll
  for (int i = 0; i < 13; ++i) { __builtin_amdgcn_sched_group_barrier(0x008, 1, 0); __builtin_amdgcn_sched_group_barrier(0x100, 1, 0); }
  __builtin_amdgcn_sched_group_barrier(0x008, 5, 0);
}
template <bool WROW>
__device__ __forceinline__ void consumeG(f32x4 (&acc)[2][9], const LAS char* lds, int w, int lane, int t0, int nb) {
  const int g = lane >> 4, q = (lane & 15) >> 2, p = lane & 3, x = 4 * (g & 1) + q;
  const unsigned offA = lane * 16;
  const unsigned offW0 = 18432 + (4 * g + q) * 256 + (((2 * w) ^ x) * 32) + (p >> 1) * 16 + (p & 1) * 8;
  const unsigned offW1 = 18432 + (4 * g + q) * 256 + (((2 * w + 1) ^ x) * 32) + (p >> 1) * 16 + (p & 1) * 8;
  FragG f0, f1;
  int slot = t0 % NS;
  BAR();
  { const LAS char* s = lds + slot * SLOT; ldG(f0, s + offA, s + offW0, s + offW1, 0); }
  for (int t = 0; t < nb; ++t) {
    { const LAS char* s = lds + slot * SLOT; ldG(f1, s + offA, s + offW0, s + offW1, 1); }
    mmaG<WROW>(acc, f0);
    schedG();
    WAIT_LGKM0();
    slot = slot + 1 == NS ? 0 : slot + 1;
    if (t + 1 < nb) {
      BAR();
      const LAS char* s = lds + slot * SLOT; ldG(f0, s + offA, s + offW0, s + offW1, 0);
    }
    mmaG<WROW>(acc, f1);
    schedG();
  }
}
template <int NA>
struct LoadG {
  unsigned va[NA], vw[4]; unsigned la, lw; rsrc_t ra, rw; unsigned astep, wstep;
  __device__ __forceinline__ void init(int l, int lane, unsigned ldsbase, rsrc_t ra_, rsrc_t rw_, unsigned ldw, unsigned astep_, unsigned wstep_) {
    ra = ra_; rw = rw_; astep = astep_; wstep = wstep_;
#pragma unroll
    for (int i = 0; i < NA; ++i) { const int a = i < 4 ? l + 4 * i : 16 + l; va[i] = (a / 9) * 36864 + (a % 9) * 1024 + lane * 16; }
#pragma unroll
    for (int i = 0; i < 4; ++i) { const int row = 4 * (l + 4 * i) + (lane >> 4), cp = lane & 15; vw[i] = row * ldw + ((cp ^ ((row & 7) << 1)) * 16); }
    la = ldsbase + l * 1024; lw = ldsbase + 18432 + l * 1024;
  }
  __device__ __forceinline__ void issue(int slot, int kb) {
    const unsigned sa = (unsigned)kb * astep, sw = (unsigned)kb * wstep, so = slot * SLOT;
#pragma unroll
    for (int i = 0; i < 4; ++i) dma(lw + so + i * 4096, vw[i], rw, sw);
#pragma unroll
    for (int i = 0; i < NA; ++i) dma(la + so + i * 4096, va[i], ra, sa);
  }
  static constexpr int N = NA + 4;
};
template <int NW>
struct LoadQ {
  unsigned va[4], vw[NW]; unsigned la, lw;
  __device__ __forceinline__ void init(int l, int lane, unsigned ldsbase) {
#pragma unroll
    for (int i = 0; i < 4; ++i) va[i] = (l + 4 * i) * 1024 + lane * 16;
#pragma unroll
    for (int i = 0; i < NW; ++i) { const int pc = i < 4 ? l + 4 * i : 16 + l; const int idx = pc * 64 + lane; vw[i] = (idx / 18) * 3456 + (idx % 18) * 16; }
    la = ldsbase + l * 1024; lw = ldsbase + 16384 + l * 1024;
  }
  __device__ __forceinline__ void issue(int slot, rsrc_t ra, unsigned sa, rsrc_t rw, unsigned sw) {
    const unsigned so = slot * SLOT;
#pragma unroll
    for (int i = 0; i < 4; ++i) dma(la + so + i * 4096, va[i], ra, sa);
#pragma unroll
    for (int i = 0; i < NW; ++i) dma(lw + so + i * 4096, vw[i], rw, sw);
  }
  static constexpr int N = NW + 4;
};
struct FragQ { bf16x8 a[2]; bf16x8 w[9]; };
__device__ __forceinline__ void ldQ(FragQ& f, const LAS char* pa, const LAS char* pw, int ks) {
#pragma unroll
  for (int n = 0; n < 2; ++n) f.a[n] = *(const LAS bf16x8*)(pa + ks * 8192 + n * 1024);
#pragma unroll
  for (int i = 0; i < 9; ++i) f.w[i] = cat8(trread(pw + ks * 9216 + i * 32), trread(pw + ks * 9216 + 4608 + i * 32));
}
template <bool WROW>
__device__ __forceinline__ void mmaQ(f32x4 (&acc)[2][9], const FragQ& f) {
#pragma unroll
  for (int i = 0; i < 9; ++i)
#pragma unroll
    for (int n = 0; n < 2; ++n) acc[n][i] = WROW ? MFMA(f.w[i], f.a[n], acc[n][i]) : MFMA(f.a[n], f.w[i], acc[n][i]);
}
template <bool WROW>
__device__ __forceinline__ void consumeQ(f32x4 (&acc)[2][9], const LAS char* lds, int w, int lane, int t0) {
  const int g = lane >> 4, q = (lane & 15) >> 2, p = lane & 3;
  const unsigned offA = 2 * w * 1024 + lane * 16, offW = 16384 + (4 * g + q) * 288 + 8 * p;
#pragma unroll
  for (int n = 0; n < 2; ++n)
#pragma unroll
    for (int i = 0; i < 9; ++i) acc[n][i] = (f32x4){0.f, 0.f, 0.f, 0.f};
  int slot = t0 % NS;
  for (int t = 0; t < 9; ++t) {
    BAR();
    const LAS char* s = lds + slot * SLOT;
    FragQ f0, f1;
    ldQ(f0, s + offA, s + offW, 0);
    ldQ(f1, s + offA, s + offW, 1);
    mmaQ<WROW>(acc, f0);
    mmaQ<WROW>(acc, f1);
    WAIT_LGKM0();
    slot = slot + 1 == NS ? 0 : slot + 1;
  }
}

struct Args {
  const u16 *x, *wqkv, *wo, *w1, *b1, *w2, *b2;
  u16* out;
  char* ws;
};

template <int NA>
__device__ __forceinline__ void k1_load_g(const Args& A, int l, int lane, unsigned ldsbase, int ft, int dtile, int rot) {
  LoadG<NA> L;
  L.init(l, lane, ldsbase, mkrsrc(A.ws + WS_H + dtile * 9216, 2359296 - dtile * 9216), mkrsrc(A.w1 + ft * 128, (unsigned)(S_ * F_ * 2 - ft * 256)), F_ * 2, 73728, 64 * F_ * 2);
  constexpr int NB = 32, N = LoadG<NA>::N;
#pragma unroll
  for (int t = 0; t < NS - 1; ++t) L.issue(t, (t + rot) & (NB - 1));
  int slot = NS - 1;
  for (int t = 0; t < NB - (NS - 1); ++t) {
    WAIT_VM(N * (NS - 2));
    BAR();
    L.issue(slot, (t + NS - 1 + rot) & (NB - 1));
    slot = slot + 1 == NS ? 0 : slot + 1;
  }
  WAIT_VM(N * 2); BAR();
  WAIT_VM(N * 1); BAR();
  WAIT_VM(0); BAR();
}
template <int NW>
__device__ __forceinline__ void k1_load_q(const Args& A, int l, int lane, unsigned ldsbase, int rb0, int rb1, int col0, int col1) {
  LoadQ<NW> L;
  L.init(l, lane, ldsbase);
  const rsrc_t ra = mkrsrc(A.ws + WS_X, 2359296), rw = mkrsrc(A.wqkv, D_ * 1728 * 2);
  constexpr int NB = 18, N = LoadQ<NW>::N;
  auto iss = [&](int slot, int t) {
    const int seg = t >= 9, kb = seg ? t - 9 : t;
    L.issue(slot, ra, (unsigned)(((seg ? rb1 : rb0) * 9 + kb) * 16384), rw, (unsigned)(kb * 64 * 3456 + (seg ? col1 : col0) * 2));
  };
#pragma unroll
  for (int t = 0; t < NS - 1; ++t) iss(t, t);
  int slot = NS - 1;
  for (int t = 0; t < NB - (NS - 1); ++t) {
    WAIT_VM(N * (NS - 2));
    BAR();
    iss(slot, t + NS - 1);
    slot = slot + 1 == NS ? 0 : slot + 1;
  }
  WAIT_VM(N * 2); BAR();
  WAIT_VM(N * 1); BAR();
  WAIT_VM(0); BAR();
}

__global__ __launch_bounds__(512, 2) void k1_gemm1_qkv(Args A) {
  __shared__ __attribute__((aligned(16))) char smem[LDS_BYTES];
  const LAS char* lds = (const LAS char*)smem;
  const unsigned ldsbase = (unsigned)(size_t)lds;
  const int tid = threadIdx.x, lane = tid & 63, wv = __builtin_amdgcn_readfirstlane(tid >> 6);
  const int b = blockIdx.x, xcd = b & 7, i = b >> 3;
  const int nG = xcd < 4 ? 20 : 16;
  if (i < nG) {
    const int ft = (xcd < 4 ? 5 * xcd : 20 + 4 * (xcd - 4)) + (i >> 2), dtile = i & 3;
    const int rot = 0;
    if (wv >= 4) {
      if (wv < 6) k1_load_g<5>(A, wv - 4, lane, ldsbase, ft, dtile, rot); else k1_load_g<4>(A, wv - 4, lane, ldsbase, ft, dtile, rot);
    } else {
      f32x4 acc[2][9];
#pragma unroll
      for (int n = 0; n < 2; ++n)
#pragma unroll
        for (int a = 0; a < 9; ++a) acc[n][a] = (f32x4){0.f, 0.f, 0.f, 0.f};
      const int g = lane >> 4;
      const int f0 = ft * 128 + 32 * wv + 4 * g;
      const u32x2 bl = *(const u32x2*)(A.b1 + f0), bh = *(const u32x2*)(A.b1 + f0 + 16);
      consumeG<true>(acc, lds, wv, lane, 0, 32);
      const float bias[8] = {__uint_as_float(bl[0] << 16), __uint_as_float(bl[0] & 0xffff0000u), __uint_as_float(bl[1] << 16), __uint_as_float(bl[1] & 0xffff0000u),
                             __uint_as_float(bh[0] << 16), __uint_as_float(bh[0] & 0xffff0000u), __uint_as_float(bh[1] << 16), __uint_as_float(bh[1] & 0xffff0000u)};
      char* U = A.ws + WS_U + ((size_t)(ft * 4 + wv) * 36 + dtile * 9) * 1024 + lane * 16;
#pragma unroll
      for (int a = 0; a < 9; ++a) {
        f32x4 u0, u1;
#pragma unroll
        for (int r = 0; r < 4; ++r) { u0[r] = gelu_tanh(acc[0][a][r] + bias[r]); u1[r] = gelu_tanh(acc[1][a][r] + bias[4 + r]); }
        *(bf16x8*)(U + a * 1024) = pack8(u0, u1);
      }
    }
    return;
  }
  const int j = xcd & 3, u = i - nG;
  const int nKV = xcd < 4 ? 6 : 10;
  if (u >= nKV) {
    const int rb0 = 2 * ((xcd < 4 ? 0 : 4) + (u - nKV));
    if (wv >= 4) {
      if (wv < 6) k1_load_q<5>(A, wv - 4, lane, ldsbase, rb0, rb0 + 1, 144 * j, 144 * j); else k1_load_q<4>(A, wv - 4, lane, ldsbase, rb0, rb0 + 1, 144 * j, 144 * j);
    } else {
      for (int seg = 0; seg < 2; ++seg) {
        f32x4 acc[2][9];
        consumeQ<true>(acc, lds, wv, lane, 9 * seg);
#pragma unroll
        for (int n = 0; n < 2; ++n) {
          char* P = A.ws + WS_PQ + ((size_t)(j * 128 + (rb0 + seg) * 8 + 2 * wv + n) * 5) * 1024 + lane * 16;
#pragma unroll
          for (int m = 0; m < 5; ++m) {
            f32x4 p0, p1;
#pragma unroll
            for (int r = 0; r < 4; ++r) { p0[r] = phi(acc[n][2 * m][r]); p1[r] = m < 4 ? phi(acc[n][m < 4 ? 2 * m + 1 : 0][r]) : 0.f; }
            *(bf16x8*)(P + m * 1024) = pack8(p0, p1);
          }
        }
      }
    }
    return;
  }
  const int rb = (xcd < 4 ? 0 : 6) + u;
  float* red = (float*)(smem + 0);
  if (wv >= 4) {
    if (wv < 6) k1_load_q<5>(A, wv - 4, lane, ldsbase, rb, rb, 576 + 144 * j, 1152 + 144 * j); else k1_load_q<4>(A, wv - 4, lane, ldsbase, rb, rb, 576 + 144 * j, 1152 + 144 * j);
    BAR();
  } else {
    bf16x8 pk[9], vv[9];
    {
      f32x4 acc[2][9];
      consumeQ<false>(acc, lds, wv, lane, 0);
#pragma unroll
      for (int c = 0; c < 9; ++c) {
        f32x4 p0, p1;
#pragma unroll
        for (int r = 0; r < 4; ++r) { p0[r] = phi(acc[0][c][r]); p1[r] = phi(acc[1][c][r]); }
        pk[c] = pack8(p0, p1);
      }
      consumeQ<false>(acc, lds, wv, lane, 9);
#pragma unroll
      for (int c = 0; c < 9; ++c) vv[c] = pack8(acc[0][c], acc[1][c]);
    }
    BAR();
    const __bf16 one = (__bf16)1.0f;
    const bf16x8 ones = {one, one, one, one, one, one, one, one};
    const int g = lane >> 4, cl = lane & 15;
    float* my = red + wv * 2736;
#pragma unroll
    for (int a = 0; a < 9; ++a) {
#pragma unroll
      for (int db = -1; db <= 1; ++db) {
        const int c = a + db;
        if (c < 0 || c > 8) continue;
        const f32x4 d = MFMA(pk[a], vv[c], ((f32x4){0.f, 0.f, 0.f, 0.f}));
        const int col = 16 * c + cl, hc = col / 18;
#pragma unroll
        for (int r = 0; r < 4; ++r) { const int row = 16 * a + 4 * g + r; if (row / 18 == hc) my[hc * 342 + (row - 18 * hc) * 18 + (col - 18 * hc)] = d[r]; }
      }
      const f32x4 z = MFMA(pk[a], ones, ((f32x4){0.f, 0.f, 0.f, 0.f}));
      if (cl == 0) {
#pragma unroll
        for (int r = 0; r < 4; ++r) { const int row = 16 * a + 4 * g + r, h = row / 18; my[h * 342 + 324 + (row - 18 * h)] = z[r]; }
      }
    }
  }
  __syncthreads();
  {
    const rsrc_t rp = mkrsrc(A.ws + WS_KVP, 700416);
    for (int e = tid; e < 684; e += 512) {
      const f32x4 a0 = *(const f32x4*)(red + e * 4), a1 = *(const f32x4*)(red + 2736 + e * 4), a2 = *(const f32x4*)(red + 2 * 2736 + e * 4), a3 = *(const f32x4*)(red + 3 * 2736 + e * 4);
      const f32x4 sum = (a0 + a1) + (a2 + a3);
      __builtin_amdgcn_raw_buffer_store_b128(__builtin_bit_cast(u32x4, sum), rp, (unsigned)((rb * 4 + j) * 2736 + e * 4) * 4, 0, 16);
    }
    WAIT_VM(0);
    __syncthreads();
    unsigned* flag = (unsigned*)(smem + XTRA);
    if (tid == 0) *flag = __hip_atomic_fetch_add((GAS unsigned*)(A.ws + WS_CNT + j * 256), 1u, __ATOMIC_RELAXED, __HIP_MEMORY_SCOPE_AGENT);
    __syncthreads();
    if (*flag != 15u) return;
    float* tot = red;
    for (int e = tid; e < 684; e += 512) {
      u32x4 v[16];
#pragma unroll
      for (int r = 0; r < 16; ++r) v[r] = __builtin_amdgcn_raw_buffer_load_b128(rp, (unsigned)((r * 4 + j) * 2736 + e * 4) * 4, 0, 16);
      f32x4 sum = __builtin_bit_cast(f32x4, v[0]);
#pragma unroll
      for (int r = 1; r < 16; ++r) sum += __builtin_bit_cast(f32x4, v[r]);
      *(f32x4*)(tot + e * 4) = sum;
    }
    __syncthreads();
    for (int e = tid; e < 34 * 64; e += 512) {
      const int fr = e >> 6, l = e & 63, pi = fr >> 1, kind = fr & 1;
      const int ii = (pi + 1) >> 1, m = (ii == 0 ? 0 : (ii - 1) >> 1) + (pi > 0 && !(pi & 1) ? 1 : 0);
      const int cp = 16 * ii + (l & 15), hp = cp / 18, g = l >> 4;
      float val[8];
#pragma unroll
      for (int jj = 0; jj < 8; ++jj) {
        const int c = 32 * m + (jj < 4 ? 4 * g + jj : 12 + 4 * g + jj), h = c / 18;
        const bool ok = c < 144 && h == hp;
        const int idx = ok ? (kind ? h * 342 + 324 + (c - 18 * h) : h * 342 + (c - 18 * h) * 18 + (cp - 18 * h)) : 0;
        val[jj] = ok ? tot[idx] : 0.f;
      }
      *(u32x4*)(A.ws + WS_KVZ + (size_t)j * 34816 + e * 16) = (u32x4){pack2(val[0], val[1]), pack2(val[2], val[3]), pack2(val[4], val[5]), pack2(val[6], val[7])};
    }
  }
}

template <int NA>
__device__ __forceinline__ void k2_load(const Args& A, int l, int lane, unsigned ldsbase, int st, int dt, int j, int rot) {
  constexpr int N = NA + 4;
  constexpr int N1 = NA, N2 = (NA == 5) ? 6 : 0;
  const int n2 = l < 3 ? 6 : 5;
  (void)N2;
  {
    const rsrc_t rk = mkrsrc(A.ws + WS_KVZ + (size_t)j * 34816, 34816);
#pragma unroll
    for (int i = 0; i < N; ++i) { const int pc = i < 8 ? l + 4 * i : 32 + l; dma(ldsbase + pc * 1024, pc * 1024 + lane * 16, rk, 0); }
    const rsrc_t ro = mkrsrc(A.wo + (size_t)(144 * j) * D_ + 144 * dt, (unsigned)((D_ - 144 * j) * D_ * 2 - 288 * dt));
#pragma unroll
    for (int i = 0; i < N1; ++i) { const int pc = i < 4 ? l + 4 * i : 16 + l; const int idx = pc * 64 + lane; dma(ldsbase + SLOT + pc * 1024, (idx / 18) * 1152 + (idx % 18) * 16, ro, 0); }
#pragma unroll
    for (int i = 0; i < 6; ++i) { int pc = l + 4 * i; pc = pc > 22 ? 22 : pc; const int idx = pc * 64 + lane; int row = 64 + idx / 18; const unsigned vo = row < 144 ? row * 1152 + (idx % 18) * 16 : 0x80000000u;
      dma(ldsbase + 2 * SLOT + pc * 1024, vo, ro, 0); }
    (void)n2;
  }
  LoadG<NA> L;
  L.init(l, lane, ldsbase, mkrsrc(A.ws + WS_U + (size_t)j * 18 * 73728 + dt * 9216, 18 * 73728 - dt * 9216),
         mkrsrc(A.w2 + (size_t)j * 1152 * S_ + st * 128, (unsigned)(1152 * S_ * 2 - st * 256)), S_ * 2, 73728, 64 * S_ * 2);
  constexpr int NB = 18;
  auto kbof = [&](int t) { int k = t + rot; return k >= NB ? k - NB : k; };
  WAIT_VM(N1 + 6); BAR();
  L.issue(3, kbof(0));
  WAIT_VM(6 + N); BAR();
  L.issue(0, kbof(1));
  WAIT_VM(2 * N); BAR();
  L.issue(1, kbof(2));
  int slot = 2;
  for (int t = 0; t < NB - 3; ++t) {
    WAIT_VM(2 * N); BAR();
    L.issue(slot, kbof(t + 3));
    slot = slot + 1 == NS ? 0 : slot + 1;
  }
  WAIT_VM(N * 2); BAR();
  WAIT_VM(N * 1); BAR();
  WAIT_VM(0); BAR();
}

__global__ __launch_bounds__(512, 2) void k2_gemm2(Args A) {
  __shared__ __attribute__((aligned(16))) char smem[LDS_BYTES];
  const LAS char* lds = (const LAS char*)smem;
  const unsigned ldsbase = (unsigned)(size_t)lds;
  const int tid = threadIdx.x, lane = tid & 63, wv = __builtin_amdgcn_readfirstlane(tid >> 6);
  const int b = blockIdx.x, xcd = b & 7, i = b >> 3;
  const int j = xcd & 3, stl = i >> 2, st = 8 * (xcd >> 2) + stl, dt = i & 3;
  const int T = st * 4 + dt;
  const int rot = ((9 * stl + 18 * dt) >> 2) % 18;
  const int g = lane >> 4;
  const rsrc_t rx = mkrsrc(A.ws + WS_XCH, 64u * 16u * 18432u);
  GAS unsigned* cnt = (GAS unsigned*)(A.ws + WS_CNT + 4 * 256);
  LAS f32x4* own = (LAS f32x4*)smem;
  if (wv >= 4) {
    if (wv < 6) k2_load<5>(A, wv - 4, lane, ldsbase, st, dt, j, rot); else k2_load<4>(A, wv - 4, lane, ldsbase, st, dt, j, rot);
    BAR();
  } else {
    f32x4 acc[2][9];
#pragma unroll
    for (int n = 0; n < 2; ++n)
#pragma unroll
      for (int a = 0; a < 9; ++a) acc[n][a] = (f32x4){0.f, 0.f, 0.f, 0.f};
    {
      bf16x8 pq[2][5];
#pragma unroll
      for (int n = 0; n < 2; ++n)
#pragma unroll
        for (int m = 0; m < 5; ++m) pq[n][m] = *(const bf16x8*)(A.ws + WS_PQ + ((size_t)(j * 128 + st * 8 + 2 * wv + n) * 5 + m) * 1024 + lane * 16);
      bf16x8 of[2][5];
      BAR();
      const LAS char* kz = lds + lane * 16;
      f32x4 o[2][2];
#pragma unroll
      for (int c = 0; c < 9; ++c) {
        const int m0 = c == 0 ? 0 : (c - 1) >> 1, p0 = c == 0 ? 0 : 2 * c - 1;
        f32x4 num[2] = {{0.f, 0.f, 0.f, 0.f}, {0.f, 0.f, 0.f, 0.f}}, den[2] = {{0.f, 0.f, 0.f, 0.f}, {0.f, 0.f, 0.f, 0.f}};
#pragma unroll
        for (int e = 0; e < (c == 0 ? 1 : 2); ++e) {
          const bf16x8 fk = *(const LAS bf16x8*)(kz + (2 * (p0 + e)) * 1024), fz = *(const LAS bf16x8*)(kz + (2 * (p0 + e) + 1) * 1024);
#pragma unroll
          for (int n = 0; n < 2; ++n) { num[n] = MFMA(fk, pq[n][m0 + e], num[n]); den[n] = MFMA(fz, pq[n][m0 + e], den[n]); }
        }
#pragma unroll
        for (int n = 0; n < 2; ++n)
#pragma unroll
          for (int r = 0; r < 4; ++r) o[n][c & 1][r] = rbf(num[n][r]) * __builtin_amdgcn_rcpf(rbf(rbf(den[n][r]) + 1e-6f));
        if (c & 1) {
#pragma unroll
          for (int n = 0; n < 2; ++n) of[n][c >> 1] = pack8(o[n][0], o[n][1]);
        }
        if (c == 8) {
#pragma unroll
          for (int n = 0; n < 2; ++n) of[n][4] = pack8(o[n][0], ((f32x4){0.f, 0.f, 0.f, 0.f}));
        }
      }
      WAIT_LGKM0();
      const int q = (lane & 15) >> 2, p = lane & 3;
      const unsigned offW = (4 * g + q) * 288 + 8 * p;
      BAR();
#pragma unroll
      for (int m = 0; m < 2; ++m)
#pragma unroll
        for (int a = 0; a < 9; ++a) {
          const LAS char* s = lds + SLOT + offW + m * 9216 + a * 32;
          const bf16x8 fw = cat8(trread(s), trread(s + 4608));
#pragma unroll
          for (int n = 0; n < 2; ++n) acc[n][a] = MFMA(fw, of[n][m], acc[n][a]);
        }
      WAIT_LGKM0();
      BAR();
      const bf16x4 z4 = {(__bf16)0.f, (__bf16)0.f, (__bf16)0.f, (__bf16)0.f};
#pragma unroll
      for (int m = 2; m < 5; ++m)
#pragma unroll
        for (int a = 0; a < 9; ++a) {
          const LAS char* s = lds + 2 * SLOT + offW + (m - 2) * 9216 + a * 32;
          const bf16x8 fw = cat8(trread(s), m < 4 ? trread(s + 4608) : z4);
#pragma unroll
          for (int n = 0; n < 2; ++n) acc[n][a] = MFMA(fw, of[n][m], acc[n][a]);
        }
      WAIT_LGKM0();
    }
    consumeG<false>(acc, lds, wv, lane, 3, 18);
    BAR();
    if (wv != j) {
      const unsigned base = (unsigned)(((T * 4 + wv) * 4 + j) * 18432) + lane * 16;
#pragma unroll
      for (int n = 0; n < 2; ++n)
#pragma unroll
        for (int a = 0; a < 9; ++a) __builtin_amdgcn_raw_buffer_store_b128(__builtin_bit_cast(u32x4, acc[n][a]), rx, base + (n * 9 + a) * 1024, 0, 16);
      WAIT_VM(0);
      if (lane == 0) __hip_atomic_fetch_add(cnt + (T * 4 + wv) * 64, 1u, __ATOMIC_RELAXED, __HIP_MEMORY_SCOPE_AGENT);
    } else {
#pragma unroll
      for (int n = 0; n < 2; ++n)
#pragma unroll
        for (int a = 0; a < 9; ++a) own[(n * 9 + a) * 64 + lane] = acc[n][a];
      WAIT_LGKM0();
    }
  }
  u32x2 xr[3]; float b2v[3];
#pragma unroll
  for (int k = 0; k < 3; ++k) {
    const int e = wv + 8 * k, ee = e < 18 ? e : 0, n = ee / 9, a = ee % 9;
    const int s = st * 128 + 32 * j + 16 * n + (lane & 15), d = dt * 144 + 16 * a + 4 * g;
    xr[k] = *(const u32x2*)(A.x + (size_t)s * D_ + d);
    b2v[k] = bf2f(A.b2[s]);
  }
  if (wv == 4) {
    unsigned spins = 0;
    while (__builtin_amdgcn_readfirstlane(__hip_atomic_load(cnt + (T * 4 + j) * 64, __ATOMIC_RELAXED, __HIP_MEMORY_SCOPE_AGENT)) < 3u) {
      __builtin_amdgcn_s_sleep(1);
      if (++spins > (1u << 22)) break;
    }
  }
  BAR();
#pragma unroll
  for (int k = 0; k < 3; ++k) {
    const int e = wv + 8 * k;
    if (e < 18) {
      const int n = e / 9, a = e % 9;
      f32x4 sum = own[e * 64 + lane];
      u32x4 pv[3];
#pragma unroll
      for (int o = 0; o < 3; ++o) { const int src = o + (o >= j ? 1 : 0); pv[o] = __builtin_amdgcn_raw_buffer_load_b128(rx, (unsigned)(((T * 4 + j) * 4 + src) * 18432 + e * 1024) + lane * 16, 0, 16); }
#pragma unroll
      for (int o = 0; o < 3; ++o) sum += __builtin_bit_cast(f32x4, pv[o]);
      const int s = st * 128 + 32 * j + 16 * n + (lane & 15), d = dt * 144 + 16 * a + 4 * g;
      const float x0 = __uint_as_float(xr[k][0] << 16), x1 = __uint_as_float(xr[k][0] & 0xffff0000u), x2 = __uint_as_float(xr[k][1] << 16), x3 = __uint_as_float(xr[k][1] & 0xffff0000u);
      *(u32x2*)(A.out + (size_t)s * D_ + d) = (u32x2){pack2(x0 + (sum[0] + b2v[k]), x1 + (sum[1] + b2v[k])), pack2(x2 + (sum[2] + b2v[k]), x3 + (sum[3] + b2v[k]))};
    }
  }
}

extern "C" void kernel_launch(void* const* d_in, const int* in_sizes, int n_in, void* d_out, int out_size, void* d_ws, size_t ws_size, hipStream_t stream) {
  Args A;
  A.x = (const u16*)d_in[0]; A.wqkv = (const u16*)d_in[1]; A.wo = (const u16*)d_in[2];
  A.w1 = (const u16*)d_in[4]; A.b1 = (const u16*)d_in[5]; A.w2 = (const u16*)d_in[6]; A.b2 = (const u16*)d_in[7];
  A.out = (u16*)d_out; A.ws = (char*)d_ws;
  k0_ln<<<256, 256, 0, stream>>>(A.x, A.ws);
  k1_gemm1_qkv<<<240, 512, 0, stream>>>(A);
  k2_gemm2<<<256, 512, 0, stream>>>(A);
}
```

```cpp
#define KROT 1
#define KWIN 8
#include <hip/hip_runtime.h>
#include <hip/hip_bf16.h>

typedef unsigned short u16;
typedef __bf16 bf16x8 __attribute__((ext_vector_type(8)));
typedef __bf16 bf16x4 __attribute__((ext_vector_type(4)));
typedef float f32x4 __attribute__((ext_vector_type(4)));
typedef float f32x2 __attribute__((ext_vector_type(2)));
typedef unsigned u32x4 __attribute__((ext_vector_type(4)));
typedef unsigned u32x2 __attribute__((ext_vector_type(2)));
#define LAS __attribute__((address_space(3)))
#define GAS __attribute__((address_space(1)))
typedef __amdgpu_buffer_rsrc_t rsrc_t;

constexpr int S_ = 2048, D_ = 576, F_ = 4608;
constexpr int NS = 4;
constexpr unsigned SLOT = 34816;
constexpr unsigned LDS_BYTES = 163840;
constexpr unsigned XTRA = NS * SLOT;

constexpr size_t WS_H = 0;
constexpr size_t WS_X = WS_H + 2359296;
constexpr size_t WS_U = WS_X + 2359296;
constexpr size_t WS_PQ = WS_U + 5308416;
constexpr size_t WS_KVP = WS_PQ + 2621440;
constexpr size_t WS_KVZ = WS_KVP + 700416;
constexpr size_t WS_CNT = WS_KVZ + 139264;
constexpr size_t WS_WQ = WS_CNT + 260 * 256;
constexpr size_t WS_WO = WS_WQ + 12 * 18 * 9 * 1024;
constexpr size_t WS_XCH = WS_WO + 16 * 45 * 1024;
constexpr int NCNT = 260;

__device__ __forceinline__ float bf2f(u16 v) { return __uint_as_float((unsigned)v << 16); }
__device__ __forceinline__ float rbf(float f) { return (float)(__bf16)f; }
__device__ __forceinline__ unsigned pack2(float lo, float hi) {
  typedef __bf16 bf16x2 __attribute__((ext_vector_type(2)));
  bf16x2 v = {(__bf16)lo, (__bf16)hi};
  return __builtin_bit_cast(unsigned, v);
}
__device__ __forceinline__ bf16x8 pack8(const f32x4& a, const f32x4& b) {
  u32x4 w = {pack2(a[0], a[1]), pack2(a[2], a[3]), pack2(b[0], b[1]), pack2(b[2], b[3])};
  return __builtin_bit_cast(bf16x8, w);
}
__device__ __forceinline__ rsrc_t mkrsrc(const void* p, unsigned bytes) { return __builtin_amdgcn_make_buffer_rsrc((void*)p, (short)0, (int)bytes, 0x00020000); }
__device__ __forceinline__ bf16x4 trread(const LAS char* p) { return __builtin_amdgcn_ds_read_tr16_b64_v4bf16((LAS bf16x4*)p); }
__device__ __forceinline__ bf16x8 cat8(bf16x4 lo, bf16x4 hi) { return __builtin_shufflevector(lo, hi, 0, 1, 2, 3, 4, 5, 6, 7); }
#define MFMA(a, b, c) __builtin_amdgcn_mfma_f32_16x16x32_bf16((a), (b), (c), 0, 0, 0)
#define FENCE() asm volatile("" ::: "memory")
#define BAR() do { FENCE(); __builtin_amdgcn_s_barrier(); FENCE(); } while (0)
#define WAIT_LGKM0() asm volatile("s_waitcnt lgkmcnt(0)" ::: "memory")
#define WAIT_VM(n) asm volatile("s_waitcnt vmcnt(%0)" ::"n"(n) : "memory")
__device__ __forceinline__ void dma(unsigned ldsaddr, unsigned voff, rsrc_t r, unsigned soff) {
  asm volatile("s_mov_b32 m0, %0\n\ts_nop 0\n\tbuffer_load_dwordx4 %1, %2, %3 offen lds" ::"s"(ldsaddr), "v"(voff), "s"(r), "s"(soff) : "memory");
}
#define STAUX 16
__device__ __forceinline__ void st16(rsrc_t r, unsigned off, u32x4 v) { __builtin_amdgcn_raw_buffer_store_b128(v, r, off, 0, STAUX); }
__device__ __forceinline__ void st16(rsrc_t r, unsigned off, bf16x8 v) { __builtin_amdgcn_raw_buffer_store_b128(__builtin_bit_cast(u32x4, v), r, off, 0, STAUX); }
__device__ __forceinline__ float phi(float t) { return (t > 0.f ? t + 1.0f : __builtin_amdgcn_exp2f(t * 1.4426950408889634f)) + 1e-6f; }
__device__ __forceinline__ float gelu_tanh(float u) {
  const float k0 = -2.0f * 0.7978845608028654f * 1.4426950408889634f, k1 = k0 * 0.044715f;
  const float z = u * (k0 + k1 * u * u);
  return u * __builtin_amdgcn_rcpf(1.0f + __builtin_amdgcn_exp2f(z));
}

#define STAMP_DECL
#define STAMP0(k, idx)
#define STAMP_FLUSH(k)
template <int C> __device__ __forceinline__ float dppf(float v) { return __int_as_float(__builtin_amdgcn_update_dpp(0, __float_as_int(v), C, 0xf, 0xf, true)); }
__device__ __forceinline__ float wave_sum(float v) {
  v += dppf<0xB1>(v); v += dppf<0x4E>(v); v += dppf<0x141>(v); v += dppf<0x140>(v);
  const auto r = __builtin_amdgcn_permlane16_swap(__float_as_uint(v), __float_as_uint(v), false, false);
  v = __uint_as_float(r[0]) + __uint_as_float(r[1]);
  const auto r2 = __builtin_amdgcn_permlane32_swap(__float_as_uint(v), __float_as_uint(v), false, false);
  return __uint_as_float(r2[0]) + __uint_as_float(r2[1]);
}
__device__ __forceinline__ void ln9(float (&v)[9]) {
  float s = 0.f;
#pragma unroll
  for (int j = 0; j < 9; ++j) s += v[j];
  const float mean = wave_sum(s) * (1.0f / D_);
  float q = 0.f;
#pragma unroll
  for (int j = 0; j < 9; ++j) { const float d = v[j] - mean; q += d * d; }
  q = wave_sum(q);
  const float mb = rbf(mean), vb = rbf(q * (1.0f / D_));
  const float rden = __builtin_amdgcn_rcpf(rbf(__builtin_amdgcn_sqrtf(rbf(vb + 1e-5f))));
#pragma unroll
  for (int j = 0; j < 9; ++j) v[j] = rbf(rbf(v[j] - mb) * rden);
}
__device__ __forceinline__ void touch_lines(const void* base, unsigned bytes, unsigned ld, int lpr_log2, int l0, int l1, int tid, int nthr) {
  const rsrc_t r = mkrsrc(base, bytes);
  unsigned d = 0;
  for (int l = l0 + tid; l < l1; l += nthr) {
    unsigned v;
    asm volatile("buffer_load_dword %0, %1, %2, 0 offen" : "=v"(v) : "v"((unsigned)(l >> lpr_log2) * ld + (unsigned)(l & ((1 << lpr_log2) - 1)) * 128u), "s"(r) : "memory");
    asm volatile("s_waitcnt vmcnt(16)" ::: "memory");
    d = v;
  }
  asm volatile("s_waitcnt vmcnt(0)" ::: "memory");
  asm volatile("" ::"v"(d));
}
#define PF_W1_ROWS 0
constexpr int K0_PF = PF_W1_ROWS ? 128 : 0;
__global__ __launch_bounds__(512) void k0_prep(const u16* __restrict__ x, const u16* __restrict__ wqkv, const u16* __restrict__ wo, const u16* __restrict__ w1, char* __restrict__ ws) {
  constexpr int RS = 1168;
  __shared__ __attribute__((aligned(16))) char sm[2 * 8 * RS];
  const int tid = threadIdx.x, lane = tid & 63, w = tid >> 6;
  const int q = (lane & 15) >> 2, p = lane & 3;
  if ((int)blockIdx.x < K0_PF) {
    const int nl = PF_W1_ROWS * 72, per = (nl + K0_PF - 1) / (K0_PF ? K0_PF : 1);
    const rsrc_t r = mkrsrc(w1, S_ * F_ * 2);
    unsigned d = 0;
    for (int l = blockIdx.x * per + tid; l < min(nl, ((int)blockIdx.x + 1) * per); l += 512) { unsigned v;
      asm volatile("buffer_load_dword %0, %1, %2, 0 offen" : "=v"(v) : "v"((unsigned)l * 128u), "s"(r) : "memory"); d = v; }
    asm volatile("s_waitcnt vmcnt(0)" ::: "memory");
    asm volatile("" ::"v"(d));
    return;
  }
  const int b = blockIdx.x - K0_PF;
  STAMP_DECL;
  STAMP0(0, 0);
  if (b >= 256) {
    const u16* src; int ld, nrows; char* dst;
    if (b < 472) { const int u = b - 256, cg = u / 18, ks = u % 18; src = wqkv + (size_t)(32 * ks) * 1728 + 144 * cg; ld = 1728; nrows = 32; dst = ws + WS_WQ + (size_t)u * 9216; }
    else { const int u = b - 472, jd = u / 5, m = u % 5, j = jd >> 2, dt = jd & 3; src = wo + (size_t)(144 * j + 32 * m) * D_ + 144 * dt; ld = D_; nrows = m < 4 ? 32 : 16; dst = ws + WS_WO + (size_t)u * 9216; }
    u32x4 v[2];
#pragma unroll
    for (int i = 0; i < 2; ++i) { const int c = tid + 512 * i, row = c / 18, ch = c % 18; v[i] = (c < 576 && row < nrows) ? *(const u32x4*)(src + (size_t)row * ld + ch * 8) : (u32x4){0u, 0u, 0u, 0u}; }
#pragma unroll
    for (int i = 0; i < 2; ++i) { const int c = tid + 512 * i; if (c < 576) *(u32x4*)(sm + c * 16) = v[i]; }
    __syncthreads();
    const LAS char* t = (const LAS char*)sm + (4 * (lane >> 4) + q) * 288 + 8 * p;
    const rsrc_t rd = mkrsrc(dst, 9216);
    for (int ct = w; ct < 9; ct += 8) st16(rd, ct * 1024 + lane * 16, cat8(trread(t + ct * 32), trread(t + 4608 + ct * 32)));
    return;
  }
  if (b == 0) for (int i = tid; i < NCNT; i += 512) *(unsigned*)(ws + WS_CNT + (size_t)i * 256) = 0u;
  const int ksi = b >> 2, gw = b & 3;
  const int tok = 32 * ksi + (w < 4 ? 4 * gw + w : 16 + 4 * gw + w - 4);
  const u16* xr = x + (size_t)tok * D_;
  float v[9];
#pragma unroll
  for (int i = 0; i < 9; ++i) v[i] = bf2f(xr[lane + 64 * i]);
  STAMP0(0, 1);
  ln9(v);
#pragma unroll
  for (int i = 0; i < 9; ++i) *(__bf16*)(sm + w * RS + (lane + 64 * i) * 2) = (__bf16)v[i];
  ln9(v);
#pragma unroll
  for (int i = 0; i < 9; ++i) *(__bf16*)(sm + 8 * RS + w * RS + (lane + 64 * i) * 2) = (__bf16)v[i];
  STAMP0(0, 2);
  __syncthreads();
  const rsrc_t rh = mkrsrc(ws + WS_H, 2359296), rx = mkrsrc(ws + WS_X, 2359296);
  {
    const LAS char* hs = (const LAS char*)sm + 8 * RS;
    for (int dt0 = 4 * w; dt0 < 36; dt0 += 32) {
      const int dt = dt0 + (lane >> 4);
      const bf16x4 lo = trread(hs + q * RS + (16 * dt + 4 * p) * 2), hi = trread(hs + (4 + q) * RS + (16 * dt + 4 * p) * 2);
      st16(rh, ((ksi * 36 + dt) * 64 + gw * 16 + (lane & 15)) * 16, cat8(lo, hi));
    }
  }
  for (int i = tid; i < 576; i += 512) {
    const int rr = i & 7, ch = i >> 3;
    const int kst = ch >> 2, g = ch & 3;
    const int t = 32 * ksi + (rr < 4 ? 4 * gw + rr : 16 + 4 * gw + rr - 4);
    const u32x2 lo = *(const u32x2*)(sm + rr * RS + (32 * kst + 4 * g) * 2), hi = *(const u32x2*)(sm + rr * RS + (32 * kst + 16 + 4 * g) * 2);
    const int rb = t >> 7, st = (t >> 4) & 7, c = t & 15;
    st16(rx, (((rb * 18 + kst) * 8 + st) * 64 + g * 16 + c) * 16, (u32x4){lo[0], lo[1], hi[0], hi[1]});
  }
  STAMP0(0, 3);
  STAMP_FLUSH(0);
}

struct FragG { bf16x8 a[9]; bf16x8 w[2]; };
__device__ __forceinline__ void ldG(FragG& f, const LAS char* pa, const LAS char* pw0, const LAS char* pw1, int ks) {
#pragma unroll
  for (int n = 0; n < 2; ++n) { const LAS char* p = (n ? pw1 : pw0) + ks * 8192; f.w[n] = cat8(trread(p), trread(p + 4096)); }
#pragma unroll
  for (int a = 0; a < 9; ++a) f.a[a] = *(const LAS bf16x8*)(pa + ks * 9216 + a * 1024);
}
#define ACCC "+v"
#define MFMA_IP(acc, a, b) asm volatile("v_mfma_f32_16x16x32_bf16 %0, %1, %2, %0" : ACCC(acc) : "v"(a), "v"(b))
#define SB() __builtin_amdgcn_sched_barrier(0)
template <bool WROW, int RD, bool LOAD>
__device__ __forceinline__ void stepG(f32x4 (&acc)[2][9], const FragG& c, FragG& n, const LAS char* pa, const LAS char* pw0, const LAS char* pw1, int ks) {
  bf16x4 lo0, hi0, lo1, hi1;
  auto rd = [&](int r) {
    if (!LOAD || r > 12) return;
    if (r == 0) lo0 = trread(pw0 + ks * 8192);
    else if (r == 1) { hi0 = trread(pw0 + ks * 8192 + 4096); n.w[0] = cat8(lo0, hi0); }
    else if (r == 2) n.a[0] = *(const LAS bf16x8*)(pa + ks * 9216);
    else if (r == 3) lo1 = trread(pw1 + ks * 8192);
    else if (r == 4) { hi1 = trread(pw1 + ks * 8192 + 4096); n.w[1] = cat8(lo1, hi1); }
    else n.a[r - 4] = *(const LAS bf16x8*)(pa + ks * 9216 + (r - 4) * 1024);
  };
#pragma unroll
  for (int i = 0; i < 18; ++i) {
    const int a = i >> 1, w = i & 1;
    if (WROW) MFMA_IP(acc[w][a], c.w[w], c.a[a]); else MFMA_IP(acc[w][a], c.a[a], c.w[w]);
    if (RD == 0) rd(i);
    else if (RD == 1) { if (i < 3) { rd(2 * i); rd(2 * i + 1); } else rd(i + 3); }
    else if (RD == 2) { if (i < 5) rd(i); else if (i < 18) { const int k = i - 5; if (k % 3 != 2) rd(5 + k - k / 3); } }
    SB();
  }
}
#define RDA 1
#define RDB 0
template <bool WROW>
__device__ __forceinline__ void consumeG(f32x4 (&acc)[2][9], const LAS char* lds, int w, int lane, int t0, int nb) {
  const int g = lane >> 4, q = (lane & 15) >> 2, p = lane & 3, x = 4 * (g & 1) + q;
  const unsigned offA = lane * 16;
  const unsigned offW0 = 18432 + (4 * g + q) * 256 + (((2 * w) ^ x) * 32) + (p >> 1) * 16 + (p & 1) * 8;
  const unsigned offW1 = 18432 + (4 * g + q) * 256 + (((2 * w + 1) ^ x) * 32) + (p >> 1) * 16 + (p & 1) * 8;
  FragG f0, f1;
  int slot = t0 % NS;
  BAR();
  const LAS char* s = lds + slot * SLOT;
  const LAS char *pa = s + offA, *pw0 = s + offW0, *pw1 = s + offW1;
  ldG(f0, pa, pw0, pw1, 0);
  SB();
  for (int t = 0; t < nb - 1; ++t) {
    stepG<WROW, RDA, true>(acc, f0, f1, pa, pw0, pw1, 1);
    WAIT_LGKM0();
    slot = slot + 1 == NS ? 0 : slot + 1;
    s = lds + slot * SLOT; pa = s + offA; pw0 = s + offW0; pw1 = s + offW1;
    BAR();
    SB();
    stepG<WROW, RDB, true>(acc, f1, f0, pa, pw0, pw1, 0);
  }
  stepG<WROW, RDA, true>(acc, f0, f1, pa, pw0, pw1, 1);
  WAIT_LGKM0();
  SB();
  stepG<WROW, 0, false>(acc, f1, f0, pa, pw0, pw1, 0);
  asm volatile("s_nop 7\n\ts_nop 7" ::: "memory");
#pragma unroll
  for (int n = 0; n < 2; ++n)
#pragma unroll
    for (int a = 0; a < 9; ++a) asm volatile("" : ACCC(acc[n][a]));
}
template <int NA>
struct LoadG {
  unsigned va[NA], vw[4]; unsigned la, lw; rsrc_t ra, rw; unsigned astep, wstep;
  __device__ __forceinline__ void init(int l, int lane, unsigned ldsbase, rsrc_t ra_, rsrc_t rw_, unsigned ldw, unsigned astep_, unsigned wstep_) {
    ra = ra_; rw = rw_; astep = astep_; wstep = wstep_;
#pragma unroll
    for (int i = 0; i < NA; ++i) { const int a = i < 4 ? l + 4 * i : 16 + l; va[i] = (a / 9) * 36864 + (a % 9) * 1024 + lane * 16; }
#pragma unroll
    for (int i = 0; i < 4; ++i) { const int row = 4 * (l + 4 * i) + (lane >> 4), cp = lane & 15; vw[i] = row * ldw + ((cp ^ ((row & 7) << 1)) * 16); }
    la = ldsbase + l * 1024; lw = ldsbase + 18432 + l * 1024;
  }
  __device__ __forceinline__ void issue(int slot, int kb) {
    const unsigned sa = (unsigned)kb * astep, sw = (unsigned)kb * wstep, so = slot * SLOT;
#pragma unroll
    for (int i = 0; i < 4; ++i) dma(lw + so + i * 4096, vw[i], rw, sw);
#pragma unroll
    for (int i = 0; i < NA; ++i) dma(la + so + i * 4096, va[i], ra, sa);
  }
  static constexpr int N = NA + 4;
};
template <int NW>
struct LoadQ {
  unsigned vo, la, lw;
  __device__ __forceinline__ void init(int l, int lane, unsigned ldsbase) { vo = l * 1024 + lane * 16; la = ldsbase + l * 1024; lw = ldsbase + 16384 + l * 1024; }
  __device__ __forceinline__ void issue(int slot, rsrc_t ra, unsigned sa, rsrc_t rw, unsigned sw) {
    const unsigned so = slot * SLOT;
#pragma unroll
    for (int i = 0; i < 4; ++i) dma(la + so + i * 4096, vo, ra, sa + i * 4096);
#pragma unroll
    for (int i = 0; i < NW; ++i) dma(lw + so + i * 4096, vo, rw, sw + i * 4096);
  }
  static constexpr int N = NW + 4;
};
struct FragQ { bf16x8 a[2]; bf16x8 w[9]; };
template <bool WROW, bool LOAD>
__device__ __forceinline__ void stepQ(f32x4 (&acc)[2][9], const FragQ& c, FragQ& n, const LAS char* pa, const LAS char* pw, int ks) {
#pragma unroll
  for (int i = 0; i < 18; ++i) {
    const int k = i >> 1, s = i & 1;
    if (WROW) MFMA_IP(acc[s][k], c.w[k], c.a[s]); else MFMA_IP(acc[s][k], c.a[s], c.w[k]);
    if (LOAD) {
      if (i == 0) n.a[0] = *(const LAS bf16x8*)(pa + ks * 8192);
      else if (i == 1) n.w[0] = *(const LAS bf16x8*)(pw + ks * 9216);
      else if (i == 2) n.a[1] = *(const LAS bf16x8*)(pa + ks * 8192 + 1024);
      else if (i <= 10) n.w[i - 2] = *(const LAS bf16x8*)(pw + ks * 9216 + (i - 2) * 1024);
    }
    SB();
  }
}
template <bool WROW, bool FIRST, bool MORE>
__device__ __forceinline__ void consumeQ(f32x4 (&acc)[2][9], FragQ& f0, const LAS char* lds, int w, int lane, int t0, int nb) {
  const unsigned offA = 2 * w * 1024 + lane * 16, offW = 16384 + lane * 16;
#pragma unroll
  for (int n = 0; n < 2; ++n)
#pragma unroll
    for (int i = 0; i < 9; ++i) acc[n][i] = (f32x4){0.f, 0.f, 0.f, 0.f};
  FragQ f1;
  int slot = t0 % NS;
  const LAS char* s = lds + slot * SLOT;
  const LAS char *pa = s + offA, *pw = s + offW;
  if (FIRST) {
    BAR();
#pragma unroll
    for (int n = 0; n < 2; ++n) f0.a[n] = *(const LAS bf16x8*)(pa + n * 1024);
#pragma unroll
    for (int i = 0; i < 9; ++i) f0.w[i] = *(const LAS bf16x8*)(pw + i * 1024);
  }
  asm volatile("s_nop 1");
  SB();
  for (int t = 0; t < nb - 1; ++t) {
    stepQ<WROW, true>(acc, f0, f1, pa, pw, 1);
    WAIT_LGKM0();
    slot = slot + 1 == NS ? 0 : slot + 1;
    s = lds + slot * SLOT; pa = s + offA; pw = s + offW;
    BAR();
    SB();
    stepQ<WROW, true>(acc, f1, f0, pa, pw, 0);
  }
  stepQ<WROW, true>(acc, f0, f1, pa, pw, 1);
  WAIT_LGKM0();
  if (MORE) {
    slot = slot + 1 == NS ? 0 : slot + 1;
    s = lds + slot * SLOT; pa = s + offA; pw = s + offW;
    BAR();
  }
  SB();
  stepQ<WROW, MORE>(acc, f1, f0, pa, pw, 0);
  asm volatile("s_nop 7\n\ts_nop 7" ::: "memory");
#pragma unroll
  for (int n = 0; n < 2; ++n)
#pragma unroll
    for (int a = 0; a < 9; ++a) asm volatile("" : ACCC(acc[n][a]));
}

struct Args {
  const u16 *x, *wqkv, *wo, *w1, *b1, *w2, *b2;
  u16* out;
  char* ws;
};

template <int NA>
__device__ __forceinline__ void k1_load_g(const Args& A, int l, int lane, unsigned ldsbase, int ft, int dtile, int rot) {
  LoadG<NA> L;
  L.init(l, lane, ldsbase, mkrsrc(A.ws + WS_H + dtile * 9216, 2359296 - dtile * 9216), mkrsrc(A.w1 + ft * 128, (unsigned)(S_ * F_ * 2 - ft * 256)), F_ * 2, 73728, 64 * F_ * 2);
  constexpr int NB = 32, N = LoadG<NA>::N;
  auto kbof = [&](int t) { return (t & ~(KWIN - 1)) | ((t + rot) & (KWIN - 1)); };
#pragma unroll
  for (int t = 0; t < NS - 1; ++t) L.issue(t, kbof(t));
  int slot = NS - 1;
  for (int t = 0; t < NB - (NS - 1); ++t) {
    WAIT_VM(N * (NS - 2));
    BAR();
    L.issue(slot, kbof(t + NS - 1));
    slot = slot + 1 == NS ? 0 : slot + 1;
  }
  WAIT_VM(N * 2); BAR();
  WAIT_VM(N * 1); BAR();
  WAIT_VM(0); BAR();
}
template <int NW>
__device__ __forceinline__ void k1_load_q(const Args& A, int l, int lane, unsigned ldsbase, int rb0, int rb1, int cg0, int cg1, int rot) {
  LoadQ<NW> L;
  L.init(l, lane, ldsbase);
  const rsrc_t ra = mkrsrc(A.ws + WS_X, 2359296), rw = mkrsrc(A.ws + WS_WQ, 12 * 18 * 9216);
  constexpr int NB = 18, N = LoadQ<NW>::N;
  auto iss = [&](int slot, int t) {
    const int seg = t >= 9; int kb = (seg ? t - 9 : t) + rot; kb = kb >= 9 ? kb - 9 : kb;
    L.issue(slot, ra, (unsigned)(((seg ? rb1 : rb0) * 9 + kb) * 16384), rw, (unsigned)(((seg ? cg1 : cg0) * 9 + kb) * 18432));
  };
#pragma unroll
  for (int t = 0; t < NS - 1; ++t) iss(t, t);
  int slot = NS - 1;
  for (int t = 0; t < NB - (NS - 1); ++t) {
    WAIT_VM(N * (NS - 2));
    BAR();
    iss(slot, t + NS - 1);
    slot = slot + 1 == NS ? 0 : slot + 1;
  }
  WAIT_VM(N * 2); BAR();
  WAIT_VM(N * 1); BAR();
  WAIT_VM(0); BAR();
}

__global__ __launch_bounds__(512, 2) void k1_gemm1_qkv(Args A) {
  __shared__ __attribute__((aligned(16))) char smem[LDS_BYTES];
  const LAS char* lds = (const LAS char*)smem;
  const unsigned ldsbase = (unsigned)(size_t)lds;
  const int tid = threadIdx.x, lane = tid & 63, wv = __builtin_amdgcn_readfirstlane(tid >> 6);
  const int b = blockIdx.x, xcd = b & 7, i = b >> 3;
  const int nG = xcd < 4 ? 20 : 16;
  STAMP_DECL;
  STAMP0(1, 0);
  if (i < nG) {
    const int ft = (xcd < 4 ? 5 * xcd : 20 + 4 * (xcd - 4)) + (i >> 2), dtile = i & 3;
    const int nft = xcd < 4 ? 5 : 4;
    const int rot = KROT ? (KWIN * dtile) / 4 + (KWIN * (i >> 2)) / nft : 0;
    const int w4 = wv & 3, g = lane >> 4;
    const int f0 = ft * 128 + 32 * w4 + 4 * g;
    const u32x2 bl = *(const u32x2*)(A.b1 + f0), bh = *(const u32x2*)(A.b1 + f0 + 16);
    const rsrc_t ru = mkrsrc(A.ws + WS_U + ((size_t)(ft * 4 + w4) * 36 + dtile * 9) * 1024, 9216);
    LAS f32x4* xfer = (LAS f32x4*)smem + w4 * 512 + lane;
    auto gelu_store = [&](int a, const f32x4& c0, const f32x4& c1) {
      const float bias[8] = {__uint_as_float(bl[0] << 16), __uint_as_float(bl[0] & 0xffff0000u), __uint_as_float(bl[1] << 16), __uint_as_float(bl[1] & 0xffff0000u),
                             __uint_as_float(bh[0] << 16), __uint_as_float(bh[0] & 0xffff0000u), __uint_as_float(bh[1] << 16), __uint_as_float(bh[1] & 0xffff0000u)};
      f32x4 u0, u1;
#pragma unroll
      for (int r = 0; r < 4; ++r) { u0[r] = gelu_tanh(c0[r] + bias[r]); u1[r] = gelu_tanh(c1[r] + bias[4 + r]); }
      st16(ru, a * 1024 + lane * 16, pack8(u0, u1));
    };
    if (wv >= 4) {
      if (wv < 6) k1_load_g<5>(A, wv - 4, lane, ldsbase, ft, dtile, rot); else k1_load_g<4>(A, wv - 4, lane, ldsbase, ft, dtile, rot);
      BAR();
      BAR();
#pragma unroll
      for (int a = 0; a < 4; ++a) { const f32x4 c0 = xfer[(2 * a) * 64], c1 = xfer[(2 * a + 1) * 64]; gelu_store(5 + a, c0, c1); }
    } else {
      f32x4 acc[2][9];
#pragma unroll
      for (int n = 0; n < 2; ++n)
#pragma unroll
        for (int a = 0; a < 9; ++a) acc[n][a] = (f32x4){0.f, 0.f, 0.f, 0.f};
      STAMP0(1, 1);
      consumeG<true>(acc, lds, wv, lane, 0, 32);
      STAMP0(1, 2);
      BAR();
#pragma unroll
      for (int a = 0; a < 4; ++a) { xfer[(2 * a) * 64] = acc[0][5 + a]; xfer[(2 * a + 1) * 64] = acc[1][5 + a]; }
      WAIT_LGKM0();
      BAR();
#pragma unroll
      for (int a = 0; a < 5; ++a) gelu_store(a, acc[0][a], acc[1][a]);
    }
    STAMP0(1, 3);
    STAMP_FLUSH(1);
    return;
  }
  const int j = xcd & 3, u = i - nG;
  const int nKV = xcd < 4 ? 6 : 10;
#define QROT 0
  const int qrot = QROT ? (u >= nKV ? (9 * (u - nKV)) / 4 : (9 * u) / nKV) : 0;
  if (u >= nKV) {
    const int rb0 = 2 * ((xcd < 4 ? 0 : 4) + (u - nKV));
    if (wv >= 4) {
      if (wv < 6) k1_load_q<5>(A, wv - 4, lane, ldsbase, rb0, rb0 + 1, j, j, qrot); else k1_load_q<4>(A, wv - 4, lane, ldsbase, rb0, rb0 + 1, j, j, qrot);
    } else {
      FragQ f0;
      f32x4 acc[2][9];
      auto epi = [&](int rb) {
#pragma unroll
        for (int n = 0; n < 2; ++n) {
          const rsrc_t P = mkrsrc(A.ws + WS_PQ + ((size_t)(j * 128 + rb * 8 + 2 * wv + n) * 5) * 1024, 5120);
#pragma unroll
          for (int m = 0; m < 5; ++m) {
            f32x4 p0, p1;
#pragma unroll
            for (int r = 0; r < 4; ++r) { p0[r] = phi(acc[n][2 * m][r]); p1[r] = m < 4 ? phi(acc[n][m < 4 ? 2 * m + 1 : 0][r]) : 0.f; }
            st16(P, m * 1024 + lane * 16, pack8(p0, p1));
          }
        }
      };
      consumeQ<true, true, true>(acc, f0, lds, wv, lane, 0, 9);
      epi(rb0);
      STAMP0(1, 1);
      consumeQ<true, false, false>(acc, f0, lds, wv, lane, 9, 9);
      epi(rb0 + 1);
      STAMP0(1, 2);
    }
    STAMP0(1, 3);
    STAMP_FLUSH(1);
    return;
  }
  const int rb = (xcd < 4 ? 0 : 6) + u;
  LAS char* fr = (LAS char*)smem;
  float* cmp = (float*)(smem + XTRA);
  if (wv >= 4) {
    if (wv < 6) k1_load_q<5>(A, wv - 4, lane, ldsbase, rb, rb, 4 + j, 8 + j, qrot); else k1_load_q<4>(A, wv - 4, lane, ldsbase, rb, rb, 4 + j, 8 + j, qrot);
    BAR();
  } else {
    bf16x8 pk[9];
    f32x4 acc[2][9];
    FragQ f0;
    consumeQ<false, true, true>(acc, f0, lds, wv, lane, 0, 9);
#pragma unroll
    for (int c = 0; c < 9; ++c) {
      f32x4 p0, p1;
#pragma unroll
      for (int r = 0; r < 4; ++r) { p0[r] = phi(acc[0][c][r]); p1[r] = phi(acc[1][c][r]); }
      pk[c] = pack8(p0, p1);
    }
    consumeQ<false, false, false>(acc, f0, lds, wv, lane, 9, 9);
    STAMP0(1, 1);
    BAR();
#pragma unroll
    for (int c = 0; c < 9; ++c) {
      *(LAS bf16x8*)(fr + (c * 4 + wv) * 1024 + lane * 16) = pk[c];
      *(LAS bf16x8*)(fr + ((9 + c) * 4 + wv) * 1024 + lane * 16) = pack8(acc[0][c], acc[1][c]);
    }
    if (wv == 0) *(LAS u32x4*)(fr + 73728 + lane * 16) = (u32x4){0x3f803f80u, 0x3f803f80u, 0x3f803f80u, 0x3f803f80u};
  }
  __syncthreads();
  STAMP0(1, 6);
  {
    const int g = lane >> 4, cl = lane & 15;
    f32x4 d[5];
    int ja[5], jc[5];
#pragma unroll
    for (int k = 0; k < 5; ++k) {
      const int jb0 = wv + 8 * k, jb = jb0 < 34 ? jb0 : 33;
      const bool isz = jb >= 25;
      const int t = jb - 2, a = isz ? jb - 25 : (jb < 2 ? 0 : 1 + t / 3), c = isz ? -1 : (jb < 2 ? jb : a - 1 + t % 3);
      ja[k] = a; jc[k] = c;
      const LAS char* pa = fr + a * 4096 + lane * 16;
      const LAS char* pb = fr + (isz ? 73728 : (9 + c) * 4096) + lane * 16;
      const int sb = isz ? 0 : 1024;
      d[k] = (f32x4){0.f, 0.f, 0.f, 0.f};
#pragma unroll
      for (int r = 0; r < 4; ++r) d[k] = MFMA(*(const LAS bf16x8*)(pa + r * 1024), *(const LAS bf16x8*)(pb + r * sb), d[k]);
    }
    STAMP0(1, 7);
#pragma unroll
    for (int k = 0; k < 5; ++k) {
      const bool isz = jc[k] < 0;
      const int col = 16 * jc[k] + cl, hc = isz ? 0 : (col * 3641) >> 16;
#pragma unroll
      for (int r = 0; r < 4; ++r) {
        const int row = 16 * ja[k] + 4 * g + r, hr = (row * 3641) >> 16;
        const bool ok = isz ? cl == 0 : hr == hc;
        const int idx = isz ? 324 * (hr + 1) + row : 18 * row + col;
        cmp[ok ? idx : 2736 + lane] = d[k][r];
      }
    }
  }
  STAMP0(1, 2);
  __syncthreads();
  {
    const rsrc_t rp = mkrsrc(A.ws + WS_KVP, 700416);
    for (int e = tid; e < 684; e += 512)
      __builtin_amdgcn_raw_buffer_store_b128(*(const u32x4*)(cmp + e * 4), rp, (unsigned)((rb * 4 + j) * 2736 + e * 4) * 4, 0, 16);
    WAIT_VM(0);
    __syncthreads();
    unsigned* flag = (unsigned*)(smem + XTRA + 16384);
    if (tid == 0) *flag = __hip_atomic_fetch_add((GAS unsigned*)(A.ws + WS_CNT + j * 256), 1u, __ATOMIC_RELAXED, __HIP_MEMORY_SCOPE_AGENT);
    __syncthreads();
    STAMP0(1, 3);
    if (*flag != 15u) { STAMP_FLUSH(1); return; }
    float* tot = (float*)smem;
    {
      const int e2 = tid < 172 ? 512 + tid : tid;
      u32x4 v[16], w2[16];
#pragma unroll
      for (int r = 0; r < 16; ++r) v[r] = __builtin_amdgcn_raw_buffer_load_b128(rp, (unsigned)((r * 4 + j) * 2736 + tid * 4) * 4, 0, 16);
#pragma unroll
      for (int r = 0; r < 16; ++r) w2[r] = __builtin_amdgcn_raw_buffer_load_b128(rp, tid < 172 ? (unsigned)((r * 4 + j) * 2736 + e2 * 4) * 4 : 0x80000000u, 0, 16);
      f32x4 sum = __builtin_bit_cast(f32x4, v[0]), sum2 = __builtin_bit_cast(f32x4, w2[0]);
#pragma unroll
      for (int r = 1; r < 16; ++r) { sum += __builtin_bit_cast(f32x4, v[r]); sum2 += __builtin_bit_cast(f32x4, w2[r]); }
      *(f32x4*)(tot + tid * 4) = sum;
      if (tid < 172) *(f32x4*)(tot + e2 * 4) = sum2;
    }
    __syncthreads();
    STAMP0(1, 4);
    for (int e = tid; e < 34 * 64; e += 512) {
      const int fi = e >> 6, l = e & 63, pi = fi >> 1, kind = fi & 1;
      const int ii = (pi + 1) >> 1, m = (ii == 0 ? 0 : (ii - 1) >> 1) + (pi > 0 && !(pi & 1) ? 1 : 0);
      const int cp = 16 * ii + (l & 15), hp = (cp * 3641) >> 16, g = l >> 4;
      const int base = kind ? 324 * (hp + 1) : cp, mul = kind ? 1 : 18;
      float val[8];
#pragma unroll
      for (int jj = 0; jj < 8; ++jj) {
        const int c = 32 * m + (jj < 4 ? 4 * g + jj : 12 + 4 * g + jj);
        const bool ok = (unsigned)(c - 18 * hp) < 18u;
        const float v = tot[ok ? base + c * mul : 0];
        val[jj] = ok ? v : 0.f;
      }
      st16(mkrsrc(A.ws + WS_KVZ + (size_t)j * 34816, 34816), e * 16, (u32x4){pack2(val[0], val[1]), pack2(val[2], val[3]), pack2(val[4], val[5]), pack2(val[6], val[7])});
    }
    STAMP0(1, 5);
    STAMP_FLUSH(1);
  }
}

template <int NA>
__device__ __forceinline__ void k2_load(const Args& A, int l, int lane, unsigned ldsbase, int st, int dt, int j, int rot) {
  constexpr int N = NA + 4;
  constexpr int N1 = 7, N2 = NA;
  {
    const unsigned vo = lane * 16;
    const rsrc_t rk = mkrsrc(A.ws + WS_KVZ + (size_t)j * 34816, 34816);
#pragma unroll
    for (int i = 0; i < N; ++i) { const int pc = i < 8 ? l + 4 * i : 32 + l; dma(ldsbase + pc * 1024, vo, rk, pc * 1024); }
    const rsrc_t ro = mkrsrc(A.ws + WS_WO + (size_t)(j * 4 + dt) * 46080, 46080);
#pragma unroll
    for (int i = 0; i < N1; ++i) { int pc = l + 4 * i; pc = pc > 26 ? 26 : pc; dma(ldsbase + SLOT + pc * 1024, vo, ro, pc * 1024); }
#pragma unroll
    for (int i = 0; i < N2; ++i) { const int pc = i < 4 ? l + 4 * i : 16 + l; dma(ldsbase + 2 * SLOT + pc * 1024, vo, ro, (27 + pc) * 1024); }
  }
  LoadG<NA> L;
  L.init(l, lane, ldsbase, mkrsrc(A.ws + WS_U + (size_t)j * 18 * 73728 + dt * 9216, 18 * 73728 - dt * 9216),
         mkrsrc(A.w2 + (size_t)j * 1152 * S_ + st * 128, (unsigned)(1152 * S_ * 2 - st * 256)), S_ * 2, 73728, 64 * S_ * 2);
  constexpr int NB = 18;
  auto kbof = [&](int t) { int k = t + rot; return k >= NB ? k - NB : k; };
  WAIT_VM(N1 + N2); BAR();
  L.issue(3, kbof(0));
  WAIT_VM(N2 + N); BAR();
  L.issue(0, kbof(1));
  WAIT_VM(2 * N); BAR();
  L.issue(1, kbof(2));
  int slot = 2;
  for (int t = 0; t < NB - 3; ++t) {
    WAIT_VM(2 * N); BAR();
    L.issue(slot, kbof(t + 3));
    slot = slot + 1 == NS ? 0 : slot + 1;
  }
  WAIT_VM(N * 2); BAR();
  WAIT_VM(N * 1); BAR();
  WAIT_VM(0); BAR();
}

__global__ __launch_bounds__(512, 2) void k2_gemm2(Args A) {
  __shared__ __attribute__((aligned(16))) char smem[LDS_BYTES];
  const LAS char* lds = (const LAS char*)smem;
  const unsigned ldsbase = (unsigned)(size_t)lds;
  const int tid = threadIdx.x, lane = tid & 63, wv = __builtin_amdgcn_readfirstlane(tid >> 6);
  const int b = blockIdx.x, xcd = b & 7, i = b >> 3;
  const int j = xcd & 3, stl = i >> 2, st = 8 * (xcd >> 2) + stl, dt = i & 3;
  const int T = st * 4 + dt;
  const int rot = ((9 * stl + 18 * dt) >> 2) % 18;
  STAMP_DECL;
  STAMP0(2, 0);
  const int g = lane >> 4;
  const rsrc_t rx = mkrsrc(A.ws + WS_XCH, 64u * 16u * 9216u);
  GAS unsigned* cnt = (GAS unsigned*)(A.ws + WS_CNT + 4 * 256);
  LAS f32x4* own = (LAS f32x4*)smem;
  if (wv >= 4) {
    if (wv < 6) k2_load<5>(A, wv - 4, lane, ldsbase, st, dt, j, rot); else k2_load<4>(A, wv - 4, lane, ldsbase, st, dt, j, rot);
    BAR();
  } else {
    f32x4 acc[2][9];
#pragma unroll
    for (int n = 0; n < 2; ++n)
#pragma unroll
      for (int a = 0; a < 9; ++a) acc[n][a] = (f32x4){0.f, 0.f, 0.f, 0.f};
    {
      bf16x8 pq[2][5];
#pragma unroll
      for (int n = 0; n < 2; ++n)
#pragma unroll
        for (int m = 0; m < 5; ++m) pq[n][m] = *(const bf16x8*)(A.ws + WS_PQ + ((size_t)(j * 128 + st * 8 + 2 * wv + n) * 5 + m) * 1024 + lane * 16);
      bf16x8 of[2][5];
      BAR();
      STAMP0(2, 1);
      const LAS char* kz = lds + lane * 16;
      f32x4 o[2][2];
#pragma unroll
      for (int c = 0; c < 9; ++c) {
        const int m0 = c == 0 ? 0 : (c - 1) >> 1, p0 = c == 0 ? 0 : 2 * c - 1;
        f32x4 num[2] = {{0.f, 0.f, 0.f, 0.f}, {0.f, 0.f, 0.f, 0.f}}, den[2] = {{0.f, 0.f, 0.f, 0.f}, {0.f, 0.f, 0.f, 0.f}};
#pragma unroll
        for (int e = 0; e < (c == 0 ? 1 : 2); ++e) {
          const bf16x8 fk = *(const LAS bf16x8*)(kz + (2 * (p0 + e)) * 1024), fz = *(const LAS bf16x8*)(kz + (2 * (p0 + e) + 1) * 1024);
#pragma unroll
          for (int n = 0; n < 2; ++n) { num[n] = MFMA(fk, pq[n][m0 + e], num[n]); den[n] = MFMA(fz, pq[n][m0 + e], den[n]); }
        }
#pragma unroll
        for (int n = 0; n < 2; ++n)
#pragma unroll
          for (int r = 0; r < 4; ++r) o[n][c & 1][r] = num[n][r] * __builtin_amdgcn_rcpf(den[n][r] + 1e-6f);
        if (c & 1) {
#pragma unroll
          for (int n = 0; n < 2; ++n) of[n][c >> 1] = pack8(o[n][0], o[n][1]);
        }
        if (c == 8) {
#pragma unroll
          for (int n = 0; n < 2; ++n) of[n][4] = pack8(o[n][0], ((f32x4){0.f, 0.f, 0.f, 0.f}));
        }
      }
      WAIT_LGKM0();
      BAR();
#pragma unroll
      for (int m = 0; m < 3; ++m)
#pragma unroll
        for (int a = 0; a < 9; ++a) {
          const bf16x8 fw = *(const LAS bf16x8*)(lds + SLOT + (m * 9 + a) * 1024 + lane * 16);
#pragma unroll
          for (int n = 0; n < 2; ++n) acc[n][a] = MFMA(fw, of[n][m], acc[n][a]);
        }
      WAIT_LGKM0();
      BAR();
#pragma unroll
      for (int m = 3; m < 5; ++m)
#pragma unroll
        for (int a = 0; a < 9; ++a) {
          const bf16x8 fw = *(const LAS bf16x8*)(lds + 2 * SLOT + ((m - 3) * 9 + a) * 1024 + lane * 16);
#pragma unroll
          for (int n = 0; n < 2; ++n) acc[n][a] = MFMA(fw, of[n][m], acc[n][a]);
        }
      WAIT_LGKM0();
      asm volatile("s_nop 7\n\ts_nop 7" ::: "memory");
    }
    STAMP0(2, 2);
    consumeG<false>(acc, lds, wv, lane, 3, 18);
    STAMP0(2, 3);
    BAR();
    if (wv != j) {
      const unsigned base = (unsigned)(((T * 4 + wv) * 4 + j) * 9216) + lane * 16;
#pragma unroll
      for (int a = 0; a < 9; ++a) __builtin_amdgcn_raw_buffer_store_b128(__builtin_bit_cast(u32x4, pack8(acc[0][a], acc[1][a])), rx, base + a * 1024, 0, 16);
      WAIT_VM(0);
      if (lane == 0) __hip_atomic_fetch_add(cnt + (T * 4 + wv) * 64, 1u, __ATOMIC_RELAXED, __HIP_MEMORY_SCOPE_AGENT);
    } else {
#pragma unroll
      for (int n = 0; n < 2; ++n)
#pragma unroll
        for (int a = 0; a < 9; ++a) own[(n * 9 + a) * 64 + lane] = acc[n][a];
      WAIT_LGKM0();
    }
  }
  const int na = wv == 0 ? 2 : 1;
  u32x2 xr[2][2]; float b2v[2];
#pragma unroll
  for (int n = 0; n < 2; ++n) {
    const int s = st * 128 + 32 * j + 16 * n + (lane & 15);
    b2v[n] = bf2f(A.b2[s]);
#pragma unroll
    for (int k = 0; k < 2; ++k) xr[k][n] = *(const u32x2*)(A.x + (size_t)s * D_ + dt * 144 + 16 * (k ? 8 : wv) + 4 * g);
  }
  STAMP0(2, 4);
  if (wv == 4) {
    unsigned spins = 0;
    while (__builtin_amdgcn_readfirstlane(__hip_atomic_load(cnt + (T * 4 + j) * 64, __ATOMIC_RELAXED, __HIP_MEMORY_SCOPE_AGENT)) < 3u) {
      __builtin_amdgcn_s_sleep(1);
      if (++spins > (1u << 22)) break;
    }
  }
  BAR();
  STAMP0(2, 5);
  u32x4 pv[2][3];
#pragma unroll
  for (int k = 0; k < 2; ++k)
#pragma unroll
    for (int o = 0; o < 3; ++o) { const int src = o + (o >= j ? 1 : 0);
      pv[k][o] = __builtin_amdgcn_raw_buffer_load_b128(rx, (k < na) ? (unsigned)(((T * 4 + j) * 4 + src) * 9216 + (k ? 8 : wv) * 1024) + lane * 16 : 0x80000000u, 0, 16); }
#pragma unroll
  for (int k = 0; k < 2; ++k) {
    if (k < na) {
      const int a = k ? 8 : wv;
#pragma unroll
      for (int n = 0; n < 2; ++n) {
        f32x4 sum = own[(n * 9 + a) * 64 + lane];
#pragma unroll
        for (int o = 0; o < 3; ++o) {
          const unsigned u0 = pv[k][o][2 * n], u1 = pv[k][o][2 * n + 1];
          sum += (f32x4){__uint_as_float(u0 << 16), __uint_as_float(u0 & 0xffff0000u), __uint_as_float(u1 << 16), __uint_as_float(u1 & 0xffff0000u)};
        }
        const int s = st * 128 + 32 * j + 16 * n + (lane & 15), d = dt * 144 + 16 * a + 4 * g;
        const unsigned y0 = xr[k][n][0], y1 = xr[k][n][1];
        const float x0 = __uint_as_float(y0 << 16), x1 = __uint_as_float(y0 & 0xffff0000u), x2 = __uint_as_float(y1 << 16), x3 = __uint_as_float(y1 & 0xffff0000u);
        *(u32x2*)(A.out + (size_t)s * D_ + d) = (u32x2){pack2(x0 + (sum[0] + b2v[n]), x1 + (sum[1] + b2v[n])), pack2(x2 + (sum[2] + b2v[n]), x3 + (sum[3] + b2v[n]))};
      }
    }
  }
  STAMP0(2, 6);
  STAMP_FLUSH(2);
}

extern "C" void kernel_launch(void* const* d_in, const int* in_sizes, int n_in, void* d_out, int out_size, void* d_ws, size_t ws_size, hipStream_t stream) {
  Args A;
  A.x = (const u16*)d_in[0]; A.wqkv = (const u16*)d_in[1]; A.wo = (const u16*)d_in[2];
  A.w1 = (const u16*)d_in[4]; A.b1 = (const u16*)d_in[5]; A.w2 = (const u16*)d_in[6]; A.b2 = (const u16*)d_in[7];
  A.out = (u16*)d_out; A.ws = (char*)d_ws;
  k0_prep<<<K0_PF + 552, 512, 0, stream>>>(A.x, A.wqkv, A.wo, A.w1, A.ws);
  k1_gemm1_qkv<<<240, 512, 0, stream>>>(A);
  k2_gemm2<<<256, 512, 0, stream>>>(A);
}
```

```cpp
#define KROT 1
#define KWIN 4
#define TLBW 2097152
#include <hip/hip_runtime.h>
#include <hip/hip_bf16.h>

typedef unsigned short u16;
typedef __bf16 bf16x8 __attribute__((ext_vector_type(8)));
typedef __bf16 bf16x4 __attribute__((ext_vector_type(4)));
typedef float f32x4 __attribute__((ext_vector_type(4)));
typedef float f32x2 __attribute__((ext_vector_type(2)));
typedef unsigned u32x4 __attribute__((ext_vector_type(4)));
typedef unsigned u32x2 __attribute__((ext_vector_type(2)));
#define LAS __attribute__((address_space(3)))
#define GAS __attribute__((address_space(1)))
typedef __amdgpu_buffer_rsrc_t rsrc_t;

constexpr int S_ = 2048, D_ = 576, F_ = 4608;
constexpr int NS = 4;
constexpr unsigned SLOT = 34816;
constexpr unsigned LDS_BYTES = 163840;
constexpr unsigned XTRA = NS * SLOT;

constexpr size_t WS_H = 0;
constexpr size_t WS_X = WS_H + 2359296;
constexpr size_t WS_U = WS_X + 2359296;
constexpr size_t WS_PQ = WS_U + 5308416;
constexpr size_t WS_KVP = WS_PQ + 2621440;
constexpr size_t WS_KVZ = WS_KVP + 700416;
constexpr size_t WS_CNT = WS_KVZ + 139264;
constexpr size_t WS_WQ = WS_CNT + 260 * 256;
constexpr size_t WS_WO = WS_WQ + 12 * 18 * 9 * 1024;
constexpr size_t WS_XCH = WS_WO + 16 * 45 * 1024;
constexpr int NCNT = 260;

__device__ __forceinline__ float bf2f(u16 v) { return __uint_as_float((unsigned)v << 16); }
__device__ __forceinline__ float rbf(float f) { return (float)(__bf16)f; }
__device__ __forceinline__ unsigned pack2(float lo, float hi) {
  typedef __bf16 bf16x2 __attribute__((ext_vector_type(2)));
  bf16x2 v = {(__bf16)lo, (__bf16)hi};
  return __builtin_bit_cast(unsigned, v);
}
__device__ __forceinline__ bf16x8 pack8(const f32x4& a, const f32x4& b) {
  u32x4 w = {pack2(a[0], a[1]), pack2(a[2], a[3]), pack2(b[0], b[1]), pack2(b[2], b[3])};
  return __builtin_bit_cast(bf16x8, w);
}
__device__ __forceinline__ rsrc_t mkrsrc(const void* p, unsigned bytes) { return __builtin_amdgcn_make_buffer_rsrc((void*)p, (short)0, (int)bytes, 0x00020000); }
__device__ __forceinline__ bf16x4 trread(const LAS char* p) { return __builtin_amdgcn_ds_read_tr16_b64_v4bf16((LAS bf16x4*)p); }
__device__ __forceinline__ bf16x8 cat8(bf16x4 lo, bf16x4 hi) { return __builtin_shufflevector(lo, hi, 0, 1, 2, 3, 4, 5, 6, 7); }
#define MFMA(a, b, c) __builtin_amdgcn_mfma_f32_16x16x32_bf16((a), (b), (c), 0, 0, 0)
#define FENCE() asm volatile("" ::: "memory")
#define BAR() do { FENCE(); __builtin_amdgcn_s_barrier(); FENCE(); } while (0)
#define WAIT_LGKM0() asm volatile("s_waitcnt lgkmcnt(0)" ::: "memory")
#define WAIT_VM(n) asm volatile("s_waitcnt vmcnt(%0)" ::"n"(n) : "memory")
__device__ __forceinline__ void dma(unsigned ldsaddr, unsigned voff, rsrc_t r, unsigned soff) {
  asm volatile("s_mov_b32 m0, %0\n\ts_nop 0\n\tbuffer_load_dwordx4 %1, %2, %3 offen lds" ::"s"(ldsaddr), "v"(voff), "s"(r), "s"(soff) : "memory");
}
#define STAUX 16
__device__ __forceinline__ void st16(rsrc_t r, unsigned off, u32x4 v) { __builtin_amdgcn_raw_buffer_store_b128(v, r, off, 0, STAUX); }
__device__ __forceinline__ void st16(rsrc_t r, unsigned off, bf16x8 v) { __builtin_amdgcn_raw_buffer_store_b128(__builtin_bit_cast(u32x4, v), r, off, 0, STAUX); }
__device__ __forceinline__ float phi(float t) { return (t > 0.f ? t + 1.0f : __builtin_amdgcn_exp2f(t * 1.4426950408889634f)) + 1e-6f; }
__device__ __forceinline__ float gelu_tanh(float u) {
  const float k0 = -2.0f * 0.7978845608028654f * 1.4426950408889634f, k1 = k0 * 0.044715f;
  const float z = u * (k0 + k1 * u * u);
  return u * __builtin_amdgcn_rcpf(1.0f + __builtin_amdgcn_exp2f(z));
}

#define STAMP_DECL
#define STAMP0(k, idx)
#define STAMP_FLUSH(k)
template <int C> __device__ __forceinline__ float dppf(float v) { return __int_as_float(__builtin_amdgcn_update_dpp(0, __float_as_int(v), C, 0xf, 0xf, true)); }
__device__ __forceinline__ float wave_sum(float v) {
  v += dppf<0xB1>(v); v += dppf<0x4E>(v); v += dppf<0x141>(v); v += dppf<0x140>(v);
  const auto r = __builtin_amdgcn_permlane16_swap(__float_as_uint(v), __float_as_uint(v), false, false);
  v = __uint_as_float(r[0]) + __uint_as_float(r[1]);
  const auto r2 = __builtin_amdgcn_permlane32_swap(__float_as_uint(v), __float_as_uint(v), false, false);
  return __uint_as_float(r2[0]) + __uint_as_float(r2[1]);
}
__device__ __forceinline__ void ln9(float (&v)[9]) {
  float s = 0.f;
#pragma unroll
  for (int j = 0; j < 9; ++j) s += v[j];
  const float mean = wave_sum(s) * (1.0f / D_);
  float q = 0.f;
#pragma unroll
  for (int j = 0; j < 9; ++j) { const float d = v[j] - mean; q += d * d; }
  q = wave_sum(q);
  const float mb = rbf(mean), vb = rbf(q * (1.0f / D_));
  const float rden = __builtin_amdgcn_rcpf(rbf(__builtin_amdgcn_sqrtf(rbf(vb + 1e-5f))));
#pragma unroll
  for (int j = 0; j < 9; ++j) v[j] = rbf(rbf(v[j] - mb) * rden);
}
__device__ __forceinline__ void touch_lines(const void* base, unsigned bytes, unsigned ld, int lpr_log2, int l0, int l1, int tid, int nthr) {
  const rsrc_t r = mkrsrc(base, bytes);
  unsigned d = 0;
  for (int l = l0 + tid; l < l1; l += nthr) {
    unsigned v;
    asm volatile("buffer_load_dword %0, %1, %2, 0 offen" : "=v"(v) : "v"((unsigned)(l >> lpr_log2) * ld + (unsigned)(l & ((1 << lpr_log2) - 1)) * 128u), "s"(r) : "memory");
    asm volatile("s_waitcnt vmcnt(16)" ::: "memory");
    d = v;
  }
  asm volatile("s_waitcnt vmcnt(0)" ::: "memory");
  asm volatile("" ::"v"(d));
}
#define PF_W1_ROWS 0
constexpr int K0_TL = TLBW ? 8 : 0;
constexpr int K0_PF = K0_TL + (PF_W1_ROWS ? 128 : 0);
__device__ __forceinline__ void touch_pages(const void* base, unsigned bytes, unsigned stride, int tid) {
  const rsrc_t r = mkrsrc(base, bytes);
  unsigned d = 0;
  for (unsigned o = tid * stride; o < bytes; o += 512 * stride) { unsigned v;
    asm volatile("buffer_load_dword %0, %1, %2, 0 offen" : "=v"(v) : "v"(o), "s"(r) : "memory"); d = v; }
  asm volatile("" ::"v"(d));
}
__global__ __launch_bounds__(512) void k0_prep(const u16* __restrict__ x, const u16* __restrict__ wqkv, const u16* __restrict__ wo, const u16* __restrict__ w1, const u16* __restrict__ w2, const u16* __restrict__ outp, char* __restrict__ ws) {
  constexpr int RS = 1168;
  __shared__ __attribute__((aligned(16))) char sm[2 * 8 * RS];
  const int tid = threadIdx.x, lane = tid & 63, w = tid >> 6;
  const int q = (lane & 15) >> 2, p = lane & 3;
  if ((int)blockIdx.x < K0_TL) {
    touch_pages(w1, S_ * F_ * 2, TLBW, tid);
    touch_pages(w2, S_ * F_ * 2, TLBW, tid);
    touch_pages(ws, (unsigned)(WS_XCH + 64u * 16u * 9216u), TLBW, tid);
    touch_pages(outp, S_ * D_ * 2, TLBW, tid);
    asm volatile("s_waitcnt vmcnt(0)" ::: "memory");
    return;
  }
  if ((int)blockIdx.x < K0_PF) {
    const int nl = PF_W1_ROWS * 72, per = (nl + 127) / 128;
    const rsrc_t r = mkrsrc(w1, S_ * F_ * 2);
    unsigned d = 0;
    for (int l = (blockIdx.x - K0_TL) * per + tid; l < min(nl, ((int)blockIdx.x - K0_TL + 1) * per); l += 512) { unsigned v;
      asm volatile("buffer_load_dword %0, %1, %2, 0 offen" : "=v"(v) : "v"((unsigned)l * 128u), "s"(r) : "memory"); d = v; }
    asm volatile("s_waitcnt vmcnt(0)" ::: "memory");
    asm volatile("" ::"v"(d));
    return;
  }
  const int b = blockIdx.x - K0_PF;
  STAMP_DECL;
  STAMP0(0, 0);
  if (b >= 256) {
    const u16* src; int ld, nrows; char* dst;
    if (b < 472) { const int u = b - 256, cg = u / 18, ks = u % 18; src = wqkv + (size_t)(32 * ks) * 1728 + 144 * cg; ld = 1728; nrows = 32; dst = ws + WS_WQ + (size_t)u * 9216; }
    else { const int u = b - 472, jd = u / 5, m = u % 5, j = jd >> 2, dt = jd & 3; src = wo + (size_t)(144 * j + 32 * m) * D_ + 144 * dt; ld = D_; nrows = m < 4 ? 32 : 16; dst = ws + WS_WO + (size_t)u * 9216; }
    u32x4 v[2];
#pragma unroll
    for (int i = 0; i < 2; ++i) { const int c = tid + 512 * i, row = c / 18, ch = c % 18; v[i] = (c < 576 && row < nrows) ? *(const u32x4*)(src + (size_t)row * ld + ch * 8) : (u32x4){0u, 0u, 0u, 0u}; }
#pragma unroll
    for (int i = 0; i < 2; ++i) { const int c = tid + 512 * i; if (c < 576) *(u32x4*)(sm + c * 16) = v[i]; }
    __syncthreads();
    const LAS char* t = (const LAS char*)sm + (4 * (lane >> 4) + q) * 288 + 8 * p;
    const rsrc_t rd = mkrsrc(dst, 9216);
    for (int ct = w; ct < 9; ct += 8) st16(rd, ct * 1024 + lane * 16, cat8(trread(t + ct * 32), trread(t + 4608 + ct * 32)));
    return;
  }
  if (b == 0) for (int i = tid; i < NCNT; i += 512) *(unsigned*)(ws + WS_CNT + (size_t)i * 256) = 0u;
  const int ksi = b >> 2, gw = b & 3;
  const int tok = 32 * ksi + (w < 4 ? 4 * gw + w : 16 + 4 * gw + w - 4);
  const u16* xr = x + (size_t)tok * D_;
  float v[9];
#pragma unroll
  for (int i = 0; i < 9; ++i) v[i] = bf2f(xr[lane + 64 * i]);
  STAMP0(0, 1);
  ln9(v);
#pragma unroll
  for (int i = 0; i < 9; ++i) *(__bf16*)(sm + w * RS + (lane + 64 * i) * 2) = (__bf16)v[i];
  ln9(v);
#pragma unroll
  for (int i = 0; i < 9; ++i) *(__bf16*)(sm + 8 * RS + w * RS + (lane + 64 * i) * 2) = (__bf16)v[i];
  STAMP0(0, 2);
  __syncthreads();
  const rsrc_t rh = mkrsrc(ws + WS_H, 2359296), rx = mkrsrc(ws + WS_X, 2359296);
  {
    const LAS char* hs = (const LAS char*)sm + 8 * RS;
    for (int dt0 = 4 * w; dt0 < 36; dt0 += 32) {
      const int dt = dt0 + (lane >> 4);
      const bf16x4 lo = trread(hs + q * RS + (16 * dt + 4 * p) * 2), hi = trread(hs + (4 + q) * RS + (16 * dt + 4 * p) * 2);
      st16(rh, ((ksi * 36 + dt) * 64 + gw * 16 + (lane & 15)) * 16, cat8(lo, hi));
    }
  }
  for (int i = tid; i < 576; i += 512) {
    const int rr = i & 7, ch = i >> 3;
    const int kst = ch >> 2, g = ch & 3;
    const int t = 32 * ksi + (rr < 4 ? 4 * gw + rr : 16 + 4 * gw + rr - 4);
    const u32x2 lo = *(const u32x2*)(sm + rr * RS + (32 * kst + 4 * g) * 2), hi = *(const u32x2*)(sm + rr * RS + (32 * kst + 16 + 4 * g) * 2);
    const int rb = t >> 7, st = (t >> 4) & 7, c = t & 15;
    st16(rx, (((rb * 18 + kst) * 8 + st) * 64 + g * 16 + c) * 16, (u32x4){lo[0], lo[1], hi[0], hi[1]});
  }
  STAMP0(0, 3);
  STAMP_FLUSH(0);
}

struct FragG { bf16x8 a[9]; bf16x8 w[2]; };
__device__ __forceinline__ void ldG(FragG& f, const LAS char* pa, const LAS char* pw0, const LAS char* pw1, int ks) {
#pragma unroll
  for (int n = 0; n < 2; ++n) { const LAS char* p = (n ? pw1 : pw0) + ks * 8192; f.w[n] = cat8(trread(p), trread(p + 4096)); }
#pragma unroll
  for (int a = 0; a < 9; ++a) f.a[a] = *(const LAS bf16x8*)(pa + ks * 9216 + a * 1024);
}
#define ACCC "+v"
#define MFMA_IP(acc, a, b) asm volatile("v_mfma_f32_16x16x32_bf16 %0, %1, %2, %0" : ACCC(acc) : "v"(a), "v"(b))
#define SB() __builtin_amdgcn_sched_barrier(0)
template <bool WROW, int RD, bool LOAD>
__device__ __forceinline__ void stepG(f32x4 (&acc)[2][9], const FragG& c, FragG& n, const LAS char* pa, const LAS char* pw0, const LAS char* pw1, int ks) {
  bf16x4 lo0, hi0, lo1, hi1;
  auto rd = [&](int r) {
    if (!LOAD || r > 12) return;
    if (r == 0) lo0 = trread(pw0 + ks * 8192);
    else if (r == 1) { hi0 = trread(pw0 + ks * 8192 + 4096); n.w[0] = cat8(lo0, hi0); }
    else if (r == 2) n.a[0] = *(const LAS bf16x8*)(pa + ks * 9216);
    else if (r == 3) lo1 = trread(pw1 + ks * 8192);
    else if (r == 4) { hi1 = trread(pw1 + ks * 8192 + 4096); n.w[1] = cat8(lo1, hi1); }
    else n.a[r - 4] = *(const LAS bf16x8*)(pa + ks * 9216 + (r - 4) * 1024);
  };
#pragma unroll
  for (int i = 0; i < 18; ++i) {
    const int a = i >> 1, w = i & 1;
    if (WROW) MFMA_IP(acc[w][a], c.w[w], c.a[a]); else MFMA_IP(acc[w][a], c.a[a], c.w[w]);
    if (RD == 0) rd(i);
    else if (RD == 1) { if (i < 3) { rd(2 * i); rd(2 * i + 1); } else rd(i + 3); }
    else if (RD == 2) { if (i < 5) rd(i); else if (i < 18) { const int k = i - 5; if (k % 3 != 2) rd(5 + k - k / 3); } }
    SB();
  }
}
#define RDA 1
#define RDB 0
template <bool WROW>
__device__ __forceinline__ void consumeG(f32x4 (&acc)[2][9], const LAS char* lds, int w, int lane, int t0, int nb) {
  const int g = lane >> 4, q = (lane & 15) >> 2, p = lane & 3, x = 4 * (g & 1) + q;
  const unsigned offA = lane * 16;
  const unsigned offW0 = 18432 + (4 * g + q) * 256 + (((2 * w) ^ x) * 32) + (p >> 1) * 16 + (p & 1) * 8;
  const unsigned offW1 = 18432 + (4 * g + q) * 256 + (((2 * w + 1) ^ x) * 32) + (p >> 1) * 16 + (p & 1) * 8;
  FragG f0, f1;
  int slot = t0 % NS;
  BAR();
  const LAS char* s = lds + slot * SLOT;
  const LAS char *pa = s + offA, *pw0 = s + offW0, *pw1 = s + offW1;
  ldG(f0, pa, pw0, pw1, 0);
  SB();
  for (int t = 0; t < nb - 1; ++t) {
    stepG<WROW, RDA, true>(acc, f0, f1, pa, pw0, pw1, 1);
    WAIT_LGKM0();
    slot = slot + 1 == NS ? 0 : slot + 1;
    s = lds + slot * SLOT; pa = s + offA; pw0 = s + offW0; pw1 = s + offW1;
    BAR();
    SB();
    stepG<WROW, RDB, true>(acc, f1, f0, pa, pw0, pw1, 0);
  }
  stepG<WROW, RDA, true>(acc, f0, f1, pa, pw0, pw1, 1);
  WAIT_LGKM0();
  SB();
  stepG<WROW, 0, false>(acc, f1, f0, pa, pw0, pw1, 0);
  asm volatile("s_nop 7\n\ts_nop 7" ::: "memory");
#pragma unroll
  for (int n = 0; n < 2; ++n)
#pragma unroll
    for (int a = 0; a < 9; ++a) asm volatile("" : ACCC(acc[n][a]));
}
template <int NA>
struct LoadG {
  unsigned va[NA], vw[4]; unsigned la, lw; rsrc_t ra, rw; unsigned astep, wstep;
  __device__ __forceinline__ void init(int l, int lane, unsigned ldsbase, rsrc_t ra_, rsrc_t rw_, unsigned ldw, unsigned astep_, unsigned wstep_) {
    ra = ra_; rw = rw_; astep = astep_; wstep = wstep_;
#pragma unroll
    for (int i = 0; i < NA; ++i) { const int a = i < 4 ? l + 4 * i : 16 + l; va[i] = (a / 9) * 36864 + (a % 9) * 1024 + lane * 16; }
#pragma unroll
    for (int i = 0; i < 4; ++i) { const int row = 4 * (l + 4 * i) + (lane >> 4), cp = lane & 15; vw[i] = row * ldw + ((cp ^ ((row & 7) << 1)) * 16); }
    la = ldsbase + l * 1024; lw = ldsbase + 18432 + l * 1024;
  }
  __device__ __forceinline__ void issue(int slot, int kb) {
    const unsigned sa = (unsigned)kb * astep, sw = (unsigned)kb * wstep, so = slot * SLOT;
#pragma unroll
    for (int i = 0; i < 4; ++i) dma(lw + so + i * 4096, vw[i], rw, sw);
#pragma unroll
    for (int i = 0; i < NA; ++i) dma(la + so + i * 4096, va[i], ra, sa);
  }
  static constexpr int N = NA + 4;
};
template <int NW>
struct LoadQ {
  unsigned vo, la, lw;
  __device__ __forceinline__ void init(int l, int lane, unsigned ldsbase) { vo = l * 1024 + lane * 16; la = ldsbase + l * 1024; lw = ldsbase + 16384 + l * 1024; }
  __device__ __forceinline__ void issue(int slot, rsrc_t ra, unsigned sa, rsrc_t rw, unsigned sw) {
    const unsigned so = slot * SLOT;
#pragma unroll
    for (int i = 0; i < 4; ++i) dma(la + so + i * 4096, vo, ra, sa + i * 4096);
#pragma unroll
    for (int i = 0; i < NW; ++i) dma(lw + so + i * 4096, vo, rw, sw + i * 4096);
  }
  static constexpr int N = NW + 4;
};
struct FragQ { bf16x8 a[2]; bf16x8 w[9]; };
template <bool WROW, bool LOAD>
__device__ __forceinline__ void stepQ(f32x4 (&acc)[2][9], const FragQ& c, FragQ& n, const LAS char* pa, const LAS char* pw, int ks) {
#pragma unroll
  for (int i = 0; i < 18; ++i) {
    const int k = i >> 1, s = i & 1;
    if (WROW) MFMA_IP(acc[s][k], c.w[k], c.a[s]); else MFMA_IP(acc[s][k], c.a[s], c.w[k]);
    if (LOAD) {
      if (i == 0) n.a[0] = *(const LAS bf16x8*)(pa + ks * 8192);
      else if (i == 1) n.w[0] = *(const LAS bf16x8*)(pw + ks * 9216);
      else if (i == 2) n.a[1] = *(const LAS bf16x8*)(pa + ks * 8192 + 1024);
      else if (i <= 10) n.w[i - 2] = *(const LAS bf16x8*)(pw + ks * 9216 + (i - 2) * 1024);
    }
    SB();
  }
}
template <bool WROW, bool FIRST, bool MORE>
__device__ __forceinline__ void consumeQ(f32x4 (&acc)[2][9], FragQ& f0, const LAS char* lds, int w, int lane, int t0, int nb) {
  const unsigned offA = 2 * w * 1024 + lane * 16, offW = 16384 + lane * 16;
#pragma unroll
  for (int n = 0; n < 2; ++n)
#pragma unroll
    for (int i = 0; i < 9; ++i) acc[n][i] = (f32x4){0.f, 0.f, 0.f, 0.f};
  FragQ f1;
  int slot = t0 % NS;
  const LAS char* s = lds + slot * SLOT;
  const LAS char *pa = s + offA, *pw = s + offW;
  if (FIRST) {
    BAR();
#pragma unroll
    for (int n = 0; n < 2; ++n) f0.a[n] = *(const LAS bf16x8*)(pa + n * 1024);
#pragma unroll
    for (int i = 0; i < 9; ++i) f0.w[i] = *(const LAS bf16x8*)(pw + i * 1024);
  }
  asm volatile("s_nop 1");
  SB();
  for (int t = 0; t < nb - 1; ++t) {
    stepQ<WROW, true>(acc, f0, f1, pa, pw, 1);
    WAIT_LGKM0();
    slot = slot + 1 == NS ? 0 : slot + 1;
    s = lds + slot * SLOT; pa = s + offA; pw = s + offW;
    BAR();
    SB();
    stepQ<WROW, true>(acc, f1, f0, pa, pw, 0);
  }
  stepQ<WROW, true>(acc, f0, f1, pa, pw, 1);
  WAIT_LGKM0();
  if (MORE) {
    slot = slot + 1 == NS ? 0 : slot + 1;
    s = lds + slot * SLOT; pa = s + offA; pw = s + offW;
    BAR();
  }
  SB();
  stepQ<WROW, MORE>(acc, f1, f0, pa, pw, 0);
  asm volatile("s_nop 7\n\ts_nop 7" ::: "memory");
#pragma unroll
  for (int n = 0; n < 2; ++n)
#pragma unroll
    for (int a = 0; a < 9; ++a) asm volatile("" : ACCC(acc[n][a]));
}

struct Args {
  const u16 *x, *wqkv, *wo, *w1, *b1, *w2, *b2;
  u16* out;
  char* ws;
};

template <int NA>
__device__ __forceinline__ void k1_load_g(const Args& A, int l, int lane, unsigned ldsbase, int ft, int dtile, int rot) {
  LoadG<NA> L;
  L.init(l, lane, ldsbase, mkrsrc(A.ws + WS_H + dtile * 9216, 2359296 - dtile * 9216), mkrsrc(A.w1 + ft * 128, (unsigned)(S_ * F_ * 2 - ft * 256)), F_ * 2, 73728, 64 * F_ * 2);
  constexpr int NB = 32, N = LoadG<NA>::N;
  auto kbof = [&](int t) { return (t & ~(KWIN - 1)) | ((t + rot) & (KWIN - 1)); };
#pragma unroll
  for (int t = 0; t < NS - 1; ++t) L.issue(t, kbof(t));
  int slot = NS - 1;
  for (int t = 0; t < NB - (NS - 1); ++t) {
    WAIT_VM(N * (NS - 2));
    BAR();
    L.issue(slot, kbof(t + NS - 1));
    slot = slot + 1 == NS ? 0 : slot + 1;
  }
  WAIT_VM(N * 2); BAR();
  WAIT_VM(N * 1); BAR();
  WAIT_VM(0); BAR();
}
template <int NW>
__device__ __forceinline__ void k1_load_q(const Args& A, int l, int lane, unsigned ldsbase, int rb0, int rb1, int cg0, int cg1, int rot) {
  LoadQ<NW> L;
  L.init(l, lane, ldsbase);
  const rsrc_t ra = mkrsrc(A.ws + WS_X, 2359296), rw = mkrsrc(A.ws + WS_WQ, 12 * 18 * 9216);
  constexpr int NB = 18, N = LoadQ<NW>::N;
  auto iss = [&](int slot, int t) {
    const int seg = t >= 9; int kb = (seg ? t - 9 : t) + rot; kb = kb >= 9 ? kb - 9 : kb;
    L.issue(slot, ra, (unsigned)(((seg ? rb1 : rb0) * 9 + kb) * 16384), rw, (unsigned)(((seg ? cg1 : cg0) * 9 + kb) * 18432));
  };
#pragma unroll
  for (int t = 0; t < NS - 1; ++t) iss(t, t);
  int slot = NS - 1;
  for (int t = 0; t < NB - (NS - 1); ++t) {
    WAIT_VM(N * (NS - 2));
    BAR();
    iss(slot, t + NS - 1);
    slot = slot + 1 == NS ? 0 : slot + 1;
  }
  WAIT_VM(N * 2); BAR();
  WAIT_VM(N * 1); BAR();
  WAIT_VM(0); BAR();
}

__global__ __launch_bounds__(512, 2) void k1_gemm1_qkv(Args A) {
  __shared__ __attribute__((aligned(16))) char smem[LDS_BYTES];
  const LAS char* lds = (const LAS char*)smem;
  const unsigned ldsbase = (unsigned)(size_t)lds;
  const int tid = threadIdx.x, lane = tid & 63, wv = __builtin_amdgcn_readfirstlane(tid >> 6);
  const int b = blockIdx.x, xcd = b & 7, i = b >> 3;
  const int nG = xcd < 4 ? 20 : 16;
  STAMP_DECL;
  STAMP0(1, 0);
  if (i < nG) {
    const int ft = (xcd < 4 ? 5 * xcd : 20 + 4 * (xcd - 4)) + (i >> 2), dtile = i & 3;
    const int nft = xcd < 4 ? 5 : 4;
    const int rot = KROT ? (KWIN * dtile) / 4 + (KWIN * (i >> 2)) / nft : 0;
    const int w4 = wv & 3, g = lane >> 4;
    const int f0 = ft * 128 + 32 * w4 + 4 * g;
    const u32x2 bl = *(const u32x2*)(A.b1 + f0), bh = *(const u32x2*)(A.b1 + f0 + 16);
    const rsrc_t ru = mkrsrc(A.ws + WS_U + ((size_t)(ft * 4 + w4) * 36 + dtile * 9) * 1024, 9216);
    LAS f32x4* xfer = (LAS f32x4*)smem + w4 * 512 + lane;
    auto gelu_store = [&](int a, const f32x4& c0, const f32x4& c1) {
      const float bias[8] = {__uint_as_float(bl[0] << 16), __uint_as_float(bl[0] & 0xffff0000u), __uint_as_float(bl[1] << 16), __uint_as_float(bl[1] & 0xffff0000u),
                             __uint_as_float(bh[0] << 16), __uint_as_float(bh[0] & 0xffff0000u), __uint_as_float(bh[1] << 16), __uint_as_float(bh[1] & 0xffff0000u)};
      f32x4 u0, u1;
#pragma unroll
      for (int r = 0; r < 4; ++r) { u0[r] = gelu_tanh(c0[r] + bias[r]); u1[r] = gelu_tanh(c1[r] + bias[4 + r]); }
      st16(ru, a * 1024 + lane * 16, pack8(u0, u1));
    };
    if (wv >= 4) {
      if (wv < 6) k1_load_g<5>(A, wv - 4, lane, ldsbase, ft, dtile, rot); else k1_load_g<4>(A, wv - 4, lane, ldsbase, ft, dtile, rot);
      BAR();
      BAR();
#pragma unroll
      for (int a = 0; a < 4; ++a) { const f32x4 c0 = xfer[(2 * a) * 64], c1 = xfer[(2 * a + 1) * 64]; gelu_store(5 + a, c0, c1); }
    } else {
      f32x4 acc[2][9];
#pragma unroll
      for (int n = 0; n < 2; ++n)
#pragma unroll
        for (int a = 0; a < 9; ++a) acc[n][a] = (f32x4){0.f, 0.f, 0.f, 0.f};
      STAMP0(1, 1);
      consumeG<true>(acc, lds, wv, lane, 0, 32);
      STAMP0(1, 2);
      BAR();
#pragma unroll
      for (int a = 0; a < 4; ++a) { xfer[(2 * a) * 64] = acc[0][5 + a]; xfer[(2 * a + 1) * 64] = acc[1][5 + a]; }
      WAIT_LGKM0();
      BAR();
#pragma unroll
      for (int a = 0; a < 5; ++a) gelu_store(a, acc[0][a], acc[1][a]);
    }
    STAMP0(1, 3);
    STAMP_FLUSH(1);
    return;
  }
  const int j = xcd & 3, u = i - nG;
  const int nKV = xcd < 4 ? 6 : 10;
#define QROT 0
  const int qrot = QROT ? (u >= nKV ? (9 * (u - nKV)) / 4 : (9 * u) / nKV) : 0;
  if (u >= nKV) {
    const int rb0 = 2 * ((xcd < 4 ? 0 : 4) + (u - nKV));
    if (wv >= 4) {
      if (wv < 6) k1_load_q<5>(A, wv - 4, lane, ldsbase, rb0, rb0 + 1, j, j, qrot); else k1_load_q<4>(A, wv - 4, lane, ldsbase, rb0, rb0 + 1, j, j, qrot);
    } else {
      FragQ f0;
      f32x4 acc[2][9];
      auto epi = [&](int rb) {
#pragma unroll
        for (int n = 0; n < 2; ++n) {
          const rsrc_t P = mkrsrc(A.ws + WS_PQ + ((size_t)(j * 128 + rb * 8 + 2 * wv + n) * 5) * 1024, 5120);
#pragma unroll
          for (int m = 0; m < 5; ++m) {
            f32x4 p0, p1;
#pragma unroll
            for (int r = 0; r < 4; ++r) { p0[r] = phi(acc[n][2 * m][r]); p1[r] = m < 4 ? phi(acc[n][m < 4 ? 2 * m + 1 : 0][r]) : 0.f; }
            st16(P, m * 1024 + lane * 16, pack8(p0, p1));
          }
        }
      };
      consumeQ<true, true, true>(acc, f0, lds, wv, lane, 0, 9);
      epi(rb0);
      STAMP0(1, 1);
      consumeQ<true, false, false>(acc, f0, lds, wv, lane, 9, 9);
      epi(rb0 + 1);
      STAMP0(1, 2);
    }
    STAMP0(1, 3);
    STAMP_FLUSH(1);
    return;
  }
  const int rb = (xcd < 4 ? 0 : 6) + u;
  LAS char* fr = (LAS char*)smem;
  float* cmp = (float*)(smem + XTRA);
  if (wv >= 4) {
    if (wv < 6) k1_load_q<5>(A, wv - 4, lane, ldsbase, rb, rb, 4 + j, 8 + j, qrot); else k1_load_q<4>(A, wv - 4, lane, ldsbase, rb, rb, 4 + j, 8 + j, qrot);
    BAR();
  } else {
    bf16x8 pk[9];
    f32x4 acc[2][9];
    FragQ f0;
    consumeQ<false, true, true>(acc, f0, lds, wv, lane, 0, 9);
#pragma unroll
    for (int c = 0; c < 9; ++c) {
      f32x4 p0, p1;
#pragma unroll
      for (int r = 0; r < 4; ++r) { p0[r] = phi(acc[0][c][r]); p1[r] = phi(acc[1][c][r]); }
      pk[c] = pack8(p0, p1);
    }
    consumeQ<false, false, false>(acc, f0, lds, wv, lane, 9, 9);
    STAMP0(1, 1);
    BAR();
#pragma unroll
    for (int c = 0; c < 9; ++c) {
      *(LAS bf16x8*)(fr + (c * 4 + wv) * 1024 + lane * 16) = pk[c];
      *(LAS bf16x8*)(fr + ((9 + c) * 4 + wv) * 1024 + lane * 16) = pack8(acc[0][c], acc[1][c]);
    }
    if (wv == 0) *(LAS u32x4*)(fr + 73728 + lane * 16) = (u32x4){0x3f803f80u, 0x3f803f80u, 0x3f803f80u, 0x3f803f80u};
  }
  __syncthreads();
  STAMP0(1, 6);
  {
    const int g = lane >> 4, cl = lane & 15;
    f32x4 d[5];
    int ja[5], jc[5];
#pragma unroll
    for (int k = 0; k < 5; ++k) {
      const int jb0 = wv + 8 * k, jb = jb0 < 34 ? jb0 : 33;
      const int isz = jb >= 25, t = jb < 2 ? 0 : jb - 2, t3 = (t * 86) >> 8;
      const int a = isz ? jb - 25 : (jb < 2 ? 0 : 1 + t3), c = isz ? -1 : (jb < 2 ? jb : t3 + t - 3 * t3);
      ja[k] = a; jc[k] = c;
    }
    bf16x8 fa[2][4], fb[2][4];
    auto ldjob = [&](int k, int buf) {
      const LAS char* pa = fr + ja[k] * 4096 + lane * 16;
      const LAS char* pb = fr + (jc[k] < 0 ? 73728 : (9 + jc[k]) * 4096) + lane * 16;
      const int sb = jc[k] < 0 ? 0 : 1024;
#pragma unroll
      for (int r = 0; r < 4; ++r) { fa[buf][r] = *(const LAS bf16x8*)(pa + r * 1024); fb[buf][r] = *(const LAS bf16x8*)(pb + r * sb); }
    };
    ldjob(0, 0);
#pragma unroll
    for (int k = 0; k < 5; ++k) {
      if (k < 4) ldjob(k + 1, (k + 1) & 1);
      d[k] = (f32x4){0.f, 0.f, 0.f, 0.f};
#pragma unroll
      for (int r = 0; r < 4; ++r) d[k] = MFMA(fa[k & 1][r], fb[k & 1][r], d[k]);
    }
    STAMP0(1, 7);
    LAS float* cm = (LAS float*)(smem + XTRA);
#pragma unroll
    for (int k = 0; k < 5; ++k) {
      const bool isz = jc[k] < 0;
      const int row0 = 16 * ja[k] + 4 * g, col = 16 * jc[k] + cl, hc = (col * 3641) >> 16;
      const int lo = isz ? (cl == 0 ? row0 - 8 : 4096) : 18 * hc;
      const int idx = isz ? 2592 + row0 : 18 * (row0 - hc) + col, step = isz ? 1 : 18;
#pragma unroll
      for (int r = 0; r < 4; ++r) cm[(unsigned)(row0 + r - lo) < 18u ? idx + r * step : 2736 + lane] = d[k][r];
    }
  }
  STAMP0(1, 2);
  __syncthreads();
  {
    const rsrc_t rp = mkrsrc(A.ws + WS_KVP, 64 * 5472);
    if (tid < 342) {
      const f32x4 lo = *(const f32x4*)(cmp + tid * 8), hi = *(const f32x4*)(cmp + tid * 8 + 4);
      __builtin_amdgcn_raw_buffer_store_b128(__builtin_bit_cast(u32x4, pack8(lo, hi)), rp, (unsigned)((rb * 4 + j) * 5472 + tid * 16), 0, 16);
    }
    WAIT_VM(0);
    __syncthreads();
    unsigned* flag = (unsigned*)(smem + XTRA + 16384);
    if (tid == 0) *flag = __hip_atomic_fetch_add((GAS unsigned*)(A.ws + WS_CNT + j * 256), 1u, __ATOMIC_RELAXED, __HIP_MEMORY_SCOPE_AGENT);
    __syncthreads();
    STAMP0(1, 3);
    if (*flag != 15u) { STAMP_FLUSH(1); return; }
    float* tot = (float*)smem;
    if (tid < 342) {
      u32x4 v[16];
#pragma unroll
      for (int r = 0; r < 16; ++r) v[r] = __builtin_amdgcn_raw_buffer_load_b128(rp, (unsigned)((r * 4 + j) * 5472 + tid * 16), 0, 16);
      f32x4 lo = {0.f, 0.f, 0.f, 0.f}, hi = {0.f, 0.f, 0.f, 0.f};
#pragma unroll
      for (int r = 0; r < 16; ++r) {
        lo += (f32x4){__uint_as_float(v[r][0] << 16), __uint_as_float(v[r][0] & 0xffff0000u), __uint_as_float(v[r][1] << 16), __uint_as_float(v[r][1] & 0xffff0000u)};
        hi += (f32x4){__uint_as_float(v[r][2] << 16), __uint_as_float(v[r][2] & 0xffff0000u), __uint_as_float(v[r][3] << 16), __uint_as_float(v[r][3] & 0xffff0000u)};
      }
      *(f32x4*)(tot + tid * 8) = lo; *(f32x4*)(tot + tid * 8 + 4) = hi;
    }
    __syncthreads();
    STAMP0(1, 4);
    const rsrc_t rz = mkrsrc(A.ws + WS_KVZ + (size_t)j * 34816, 34816);
#pragma unroll
    for (int k = 0; k < 5; ++k) {
      const int fi = wv + 8 * k;
      if (fi < 34) {
        const int pi = fi >> 1, kind = fi & 1;
        const int ii = (pi + 1) >> 1, m = (ii == 0 ? 0 : (ii - 1) >> 1) + (pi > 0 && !(pi & 1) ? 1 : 0);
        const int cp = 16 * ii + (lane & 15), hp = (cp * 3641) >> 16, g = lane >> 4;
        const int c0 = 32 * m + 4 * g, mul = kind ? 1 : 18;
        const int base = kind ? 2592 + c0 : 18 * (c0 - hp) + cp, rel = c0 - 18 * hp;
        float val[8];
#pragma unroll
        for (int jj = 0; jj < 8; ++jj) {
          const int o = jj < 4 ? jj : 12 + jj;
          const bool ok = (unsigned)(rel + o) < 18u;
          const float v = tot[ok ? base + o * mul : 0];
          val[jj] = ok ? v : 0.f;
        }
        st16(rz, fi * 1024 + lane * 16, (u32x4){pack2(val[0], val[1]), pack2(val[2], val[3]), pack2(val[4], val[5]), pack2(val[6], val[7])});
      }
    }
    STAMP0(1, 5);
    STAMP_FLUSH(1);
  }
}

template <int NA>
__device__ __forceinline__ void k2_load(const Args& A, int l, int lane, unsigned ldsbase, int st, int dt, int j, int rot) {
  constexpr int N = NA + 4;
  constexpr int N1 = 7, N2 = NA;
  {
    const unsigned vo = lane * 16;
    const rsrc_t rk = mkrsrc(A.ws + WS_KVZ + (size_t)j * 34816, 34816);
#pragma unroll
    for (int i = 0; i < N; ++i) { const int pc = i < 8 ? l + 4 * i : 32 + l; dma(ldsbase + pc * 1024, vo, rk, pc * 1024); }
    const rsrc_t ro = mkrsrc(A.ws + WS_WO + (size_t)(j * 4 + dt) * 46080, 46080);
#pragma unroll
    for (int i = 0; i < N1; ++i) { int pc = l + 4 * i; pc = pc > 26 ? 26 : pc; dma(ldsbase + SLOT + pc * 1024, vo, ro, pc * 1024); }
#pragma unroll
    for (int i = 0; i < N2; ++i) { const int pc = i < 4 ? l + 4 * i : 16 + l; dma(ldsbase + 2 * SLOT + pc * 1024, vo, ro, (27 + pc) * 1024); }
  }
  LoadG<NA> L;
  L.init(l, lane, ldsbase, mkrsrc(A.ws + WS_U + (size_t)j * 18 * 73728 + dt * 9216, 18 * 73728 - dt * 9216),
         mkrsrc(A.w2 + (size_t)j * 1152 * S_ + st * 128, (unsigned)(1152 * S_ * 2 - st * 256)), S_ * 2, 73728, 64 * S_ * 2);
  constexpr int NB = 18;
  auto kbof = [&](int t) { int k = t + rot; return k >= NB ? k - NB : k; };
  WAIT_VM(N1 + N2); BAR();
  L.issue(3, kbof(0));
  WAIT_VM(N2 + N); BAR();
  L.issue(0, kbof(1));
  WAIT_VM(2 * N); BAR();
  L.issue(1, kbof(2));
  int slot = 2;
  for (int t = 0; t < NB - 3; ++t) {
    WAIT_VM(2 * N); BAR();
    L.issue(slot, kbof(t + 3));
    slot = slot + 1 == NS ? 0 : slot + 1;
  }
  WAIT_VM(N * 2); BAR();
  WAIT_VM(N * 1); BAR();
  WAIT_VM(0); BAR();
}

__global__ __launch_bounds__(512, 2) void k2_gemm2(Args A) {
  __shared__ __attribute__((aligned(16))) char smem[LDS_BYTES];
  const LAS char* lds = (const LAS char*)smem;
  const unsigned ldsbase = (unsigned)(size_t)lds;
  const int tid = threadIdx.x, lane = tid & 63, wv = __builtin_amdgcn_readfirstlane(tid >> 6);
  const int b = blockIdx.x, xcd = b & 7, i = b >> 3;
  const int j = xcd & 3, stl = i >> 2, st = 8 * (xcd >> 2) + stl, dt = i & 3;
  const int T = st * 4 + dt;
  const int rot = ((9 * stl + 18 * dt) >> 2) % 18;
  STAMP_DECL;
  STAMP0(2, 0);
  const int g = lane >> 4;
  const rsrc_t rx = mkrsrc(A.ws + WS_XCH, 64u * 16u * 9216u);
  GAS unsigned* cnt = (GAS unsigned*)(A.ws + WS_CNT + 4 * 256);
  LAS f32x4* own = (LAS f32x4*)smem;
  if (wv >= 4) {
    if (wv < 6) k2_load<5>(A, wv - 4, lane, ldsbase, st, dt, j, rot); else k2_load<4>(A, wv - 4, lane, ldsbase, st, dt, j, rot);
    BAR();
  } else {
    f32x4 acc[2][9];
#pragma unroll
    for (int n = 0; n < 2; ++n)
#pragma unroll
      for (int a = 0; a < 9; ++a) acc[n][a] = (f32x4){0.f, 0.f, 0.f, 0.f};
    {
      bf16x8 pq[2][5];
#pragma unroll
      for (int n = 0; n < 2; ++n)
#pragma unroll
        for (int m = 0; m < 5; ++m) pq[n][m] = *(const bf16x8*)(A.ws + WS_PQ + ((size_t)(j * 128 + st * 8 + 2 * wv + n) * 5 + m) * 1024 + lane * 16);
      bf16x8 of[2][5];
      BAR();
      STAMP0(2, 1);
      const LAS char* kz = lds + lane * 16;
      f32x4 o[2][2];
#pragma unroll
      for (int c = 0; c < 9; ++c) {
        const int m0 = c == 0 ? 0 : (c - 1) >> 1, p0 = c == 0 ? 0 : 2 * c - 1;
        f32x4 num[2] = {{0.f, 0.f, 0.f, 0.f}, {0.f, 0.f, 0.f, 0.f}}, den[2] = {{0.f, 0.f, 0.f, 0.f}, {0.f, 0.f, 0.f, 0.f}};
#pragma unroll
        for (int e = 0; e < (c == 0 ? 1 : 2); ++e) {
          const bf16x8 fk = *(const LAS bf16x8*)(kz + (2 * (p0 + e)) * 1024), fz = *(const LAS bf16x8*)(kz + (2 * (p0 + e) + 1) * 1024);
#pragma unroll
          for (int n = 0; n < 2; ++n) { num[n] = MFMA(fk, pq[n][m0 + e], num[n]); den[n] = MFMA(fz, pq[n][m0 + e], den[n]); }
        }
#pragma unroll
        for (int n = 0; n < 2; ++n)
#pragma unroll
          for (int r = 0; r < 4; ++r) o[n][c & 1][r] = num[n][r] * __builtin_amdgcn_rcpf(den[n][r] + 1e-6f);
        if (c & 1) {
#pragma unroll
          for (int n = 0; n < 2; ++n) of[n][c >> 1] = pack8(o[n][0], o[n][1]);
        }
        if (c == 8) {
#pragma unroll
          for (int n = 0; n < 2; ++n) of[n][4] = pack8(o[n][0], ((f32x4){0.f, 0.f, 0.f, 0.f}));
        }
      }
      WAIT_LGKM0();
      BAR();
#pragma unroll
      for (int m = 0; m < 3; ++m)
#pragma unroll
        for (int a = 0; a < 9; ++a) {
          const bf16x8 fw = *(const LAS bf16x8*)(lds + SLOT + (m * 9 + a) * 1024 + lane * 16);
#pragma unroll
          for (int n = 0; n < 2; ++n) acc[n][a] = MFMA(fw, of[n][m], acc[n][a]);
        }
      WAIT_LGKM0();
      BAR();
#pragma unroll
      for (int m = 3; m < 5; ++m)
#pragma unroll
        for (int a = 0; a < 9; ++a) {
          const bf16x8 fw = *(const LAS bf16x8*)(lds + 2 * SLOT + ((m - 3) * 9 + a) * 1024 + lane * 16);
#pragma unroll
          for (int n = 0; n < 2; ++n) acc[n][a] = MFMA(fw, of[n][m], acc[n][a]);
        }
      WAIT_LGKM0();
      asm volatile("s_nop 7\n\ts_nop 7" ::: "memory");
    }
    STAMP0(2, 2);
    consumeG<false>(acc, lds, wv, lane, 3, 18);
    STAMP0(2, 3);
    BAR();
    if (wv != j) {
      const unsigned base = (unsigned)(((T * 4 + wv) * 4 + j) * 9216) + lane * 16;
#pragma unroll
      for (int a = 0; a < 9; ++a) __builtin_amdgcn_raw_buffer_store_b128(__builtin_bit_cast(u32x4, pack8(acc[0][a], acc[1][a])), rx, base + a * 1024, 0, 16);
      WAIT_VM(0);
      if (lane == 0) __hip_atomic_fetch_add(cnt + (T * 4 + wv) * 64, 1u, __ATOMIC_RELAXED, __HIP_MEMORY_SCOPE_AGENT);
    } else {
#pragma unroll
      for (int n = 0; n < 2; ++n)
#pragma unroll
        for (int a = 0; a < 9; ++a) own[(n * 9 + a) * 64 + lane] = acc[n][a];
      WAIT_LGKM0();
    }
  }
  const int na = wv == 0 ? 2 : 1;
  u32x2 xr[2][2]; float b2v[2];
#pragma unroll
  for (int n = 0; n < 2; ++n) {
    const int s = st * 128 + 32 * j + 16 * n + (lane & 15);
    b2v[n] = bf2f(A.b2[s]);
#pragma unroll
    for (int k = 0; k < 2; ++k) xr[k][n] = *(const u32x2*)(A.x + (size_t)s * D_ + dt * 144 + 16 * (k ? 8 : wv) + 4 * g);
  }
  STAMP0(2, 4);
  if (wv == 4) {
    unsigned spins = 0;
    while (__builtin_amdgcn_readfirstlane(__hip_atomic_load(cnt + (T * 4 + j) * 64, __ATOMIC_RELAXED, __HIP_MEMORY_SCOPE_AGENT)) < 3u) {
      __builtin_amdgcn_s_sleep(1);
      if (++spins > (1u << 22)) break;
    }
  }
  BAR();
  STAMP0(2, 5);
  u32x4 pv[2][3];
#pragma unroll
  for (int k = 0; k < 2; ++k)
#pragma unroll
    for (int o = 0; o < 3; ++o) { const int src = o + (o >= j ? 1 : 0);
      pv[k][o] = __builtin_amdgcn_raw_buffer_load_b128(rx, (k < na) ? (unsigned)(((T * 4 + j) * 4 + src) * 9216 + (k ? 8 : wv) * 1024) + lane * 16 : 0x80000000u, 0, 16); }
#pragma unroll
  for (int k = 0; k < 2; ++k) {
    if (k < na) {
      const int a = k ? 8 : wv;
#pragma unroll
      for (int n = 0; n < 2; ++n) {
        f32x4 sum = own[(n * 9 + a) * 64 + lane];
#pragma unroll
        for (int o = 0; o < 3; ++o) {
          const unsigned u0 = pv[k][o][2 * n], u1 = pv[k][o][2 * n + 1];
          sum += (f32x4){__uint_as_float(u0 << 16), __uint_as_float(u0 & 0xffff0000u), __uint_as_float(u1 << 16), __uint_as_float(u1 & 0xffff0000u)};
        }
        const int s = st * 128 + 32 * j + 16 * n + (lane & 15), d = dt * 144 + 16 * a + 4 * g;
        const unsigned y0 = xr[k][n][0], y1 = xr[k][n][1];
        const float x0 = __uint_as_float(y0 << 16), x1 = __uint_as_float(y0 & 0xffff0000u), x2 = __uint_as_float(y1 << 16), x3 = __uint_as_float(y1 & 0xffff0000u);
        *(u32x2*)(A.out + (size_t)s * D_ + d) = (u32x2){pack2(x0 + (sum[0] + b2v[n]), x1 + (sum[1] + b2v[n])), pack2(x2 + (sum[2] + b2v[n]), x3 + (sum[3] + b2v[n]))};
      }
    }
  }
  STAMP0(2, 6);
  STAMP_FLUSH(2);
}

extern "C" void kernel_launch(void* const* d_in, const int* in_sizes, int n_in, void* d_out, int out_size, void* d_ws, size_t ws_size, hipStream_t stream) {
  Args A;
  A.x = (const u16*)d_in[0]; A.wqkv = (const u16*)d_in[1]; A.wo = (const u16*)d_in[2];
  A.w1 = (const u16*)d_in[4]; A.b1 = (const u16*)d_in[5]; A.w2 = (const u16*)d_in[6]; A.b2 = (const u16*)d_in[7];
  A.out = (u16*)d_out; A.ws = (char*)d_ws;
  k0_prep<<<K0_PF + 552, 512, 0, stream>>>(A.x, A.wqkv, A.wo, A.w1, A.w2, A.out, A.ws);
  k1_gemm1_qkv<<<240, 512, 0, stream>>>(A);
  k2_gemm2<<<256, 512, 0, stream>>>(A);
}
```

```cpp
#include <hip/hip_runtime.h>
#include <hip/hip_bf16.h>

typedef unsigned short u16;
typedef __bf16 bf16x8 __attribute__((ext_vector_type(8)));
typedef __bf16 bf16x4 __attribute__((ext_vector_type(4)));
typedef float f32x4 __attribute__((ext_vector_type(4)));
typedef float f32x2 __attribute__((ext_vector_type(2)));
typedef unsigned u32x4 __attribute__((ext_vector_type(4)));
typedef unsigned u32x2 __attribute__((ext_vector_type(2)));
#define LAS __attribute__((address_space(3)))
#define GAS __attribute__((address_space(1)))
typedef __amdgpu_buffer_rsrc_t rsrc_t;

constexpr int S_ = 2048, D_ = 576, F_ = 4608;
constexpr int NS = 4;
constexpr unsigned SLOT = 34816;
constexpr unsigned LDS_BYTES = 163840;
constexpr unsigned XTRA = NS * SLOT;

constexpr size_t WS_H = 0;
constexpr size_t WS_X = WS_H + 2359296;
constexpr size_t WS_U = WS_X + 2359296;
constexpr size_t WS_PQ = WS_U + 5308416;
constexpr size_t WS_O = WS_PQ + 2621440;
constexpr size_t WS_KVP = WS_O + 2621440;
constexpr size_t WS_KVZ = WS_KVP + 64 * 5472;
constexpr size_t WS_WQ = WS_KVZ + 139264;
constexpr size_t WS_WO = WS_WQ + 12 * 18 * 9 * 1024;
constexpr size_t WS_XCH = WS_WO + 16 * 45 * 1024;
constexpr size_t WS_CNT = WS_XCH + 64 * 16 * 9216;
constexpr int C_KVC = 0, C_KF = 4, C_PQF = 8, C_UF = 40, C_OC = 184, C_SR = 248, NCNT = 249;
constexpr unsigned SPIN_CAP = 1u << 15;
#define XSLEEP 3

__device__ __forceinline__ float bf2f(u16 v) { return __uint_as_float((unsigned)v << 16); }
__device__ __forceinline__ float rbf(float f) { return (float)(__bf16)f; }
__device__ __forceinline__ unsigned pack2(float lo, float hi) {
  typedef __bf16 bf16x2 __attribute__((ext_vector_type(2)));
  bf16x2 v = {(__bf16)lo, (__bf16)hi};
  return __builtin_bit_cast(unsigned, v);
}
__device__ __forceinline__ bf16x8 pack8(const f32x4& a, const f32x4& b) {
  u32x4 w = {pack2(a[0], a[1]), pack2(a[2], a[3]), pack2(b[0], b[1]), pack2(b[2], b[3])};
  return __builtin_bit_cast(bf16x8, w);
}
__device__ __forceinline__ f32x4 unpk_lo(const u32x4& v) { return (f32x4){__uint_as_float(v[0] << 16), __uint_as_float(v[0] & 0xffff0000u), __uint_as_float(v[1] << 16), __uint_as_float(v[1] & 0xffff0000u)}; }
__device__ __forceinline__ f32x4 unpk_hi(const u32x4& v) { return (f32x4){__uint_as_float(v[2] << 16), __uint_as_float(v[2] & 0xffff0000u), __uint_as_float(v[3] << 16), __uint_as_float(v[3] & 0xffff0000u)}; }
__device__ __forceinline__ rsrc_t mkrsrc(const void* p, unsigned bytes) { return __builtin_amdgcn_make_buffer_rsrc((void*)p, (short)0, (int)bytes, 0x00020000); }
__device__ __forceinline__ bf16x4 trread(const LAS char* p) { return __builtin_amdgcn_ds_read_tr16_b64_v4bf16((LAS bf16x4*)p); }
__device__ __forceinline__ bf16x8 cat8(bf16x4 lo, bf16x4 hi) { return __builtin_shufflevector(lo, hi, 0, 1, 2, 3, 4, 5, 6, 7); }
__device__ __forceinline__ bf16x8 ldsb(const LAS char* p) { return *(const LAS bf16x8*)p; }
#define MFMA(a, b, c) __builtin_amdgcn_mfma_f32_16x16x32_bf16((a), (b), (c), 0, 0, 0)
#define MFMA_IP(acc, a, b) asm volatile("v_mfma_f32_16x16x32_bf16 %0, %1, %2, %0" : "+v"(acc) : "v"(a), "v"(b))
#define SB() __builtin_amdgcn_sched_barrier(0)
#define FENCE() asm volatile("" ::: "memory")
#define BAR() do { FENCE(); __builtin_amdgcn_s_barrier(); FENCE(); } while (0)
#define WAIT_LGKM0() asm volatile("s_waitcnt lgkmcnt(0)" ::: "memory")
#define WAIT_VM(n) asm volatile("s_waitcnt vmcnt(%0)" ::"n"(n) : "memory")
template <bool SC1>
__device__ __forceinline__ void dma(unsigned ldsaddr, unsigned voff, rsrc_t r, unsigned soff) {
  if (SC1) asm volatile("s_mov_b32 m0, %0\n\ts_nop 0\n\tbuffer_load_dwordx4 %1, %2, %3 offen sc1 lds" ::"s"(ldsaddr), "v"(voff), "s"(r), "s"(soff) : "memory");
  else asm volatile("s_mov_b32 m0, %0\n\ts_nop 0\n\tbuffer_load_dwordx4 %1, %2, %3 offen lds" ::"s"(ldsaddr), "v"(voff), "s"(r), "s"(soff) : "memory");
}
__device__ __forceinline__ void st16(rsrc_t r, unsigned off, u32x4 v) { __builtin_amdgcn_raw_buffer_store_b128(v, r, off, 0, 16); }
__device__ __forceinline__ void st16(rsrc_t r, unsigned off, bf16x8 v) { __builtin_amdgcn_raw_buffer_store_b128(__builtin_bit_cast(u32x4, v), r, off, 0, 16); }
__device__ __forceinline__ u32x4 ld16(rsrc_t r, unsigned off) { return __builtin_amdgcn_raw_buffer_load_b128(r, off, 0, 16); }
__device__ __forceinline__ bf16x8 ld16b(rsrc_t r, unsigned off) { return __builtin_bit_cast(bf16x8, __builtin_amdgcn_raw_buffer_load_b128(r, off, 0, 16)); }
__device__ __forceinline__ GAS unsigned* cntp(char* ws, int i) { return (GAS unsigned*)(ws + WS_CNT + (size_t)i * 256); }
__device__ __forceinline__ unsigned ld_flag(GAS unsigned* p) { return __hip_atomic_load(p, __ATOMIC_RELAXED, __HIP_MEMORY_SCOPE_AGENT); }
__device__ __forceinline__ void st_flag(GAS unsigned* p) { __hip_atomic_store(p, 1u, __ATOMIC_RELAXED, __HIP_MEMORY_SCOPE_AGENT); }
__device__ __forceinline__ unsigned add_cnt(GAS unsigned* p) { return __hip_atomic_fetch_add(p, 1u, __ATOMIC_RELAXED, __HIP_MEMORY_SCOPE_AGENT); }
__device__ __forceinline__ unsigned poll_issue(GAS unsigned* p) { unsigned v; asm volatile("global_load_dword %0, %1, off sc1" : "=v"(v) : "v"(p) : "memory"); return v; }
__device__ __forceinline__ void wait_words(GAS unsigned* p, unsigned want);
__device__ __forceinline__ void poll_check(unsigned v, GAS unsigned* p, unsigned want) {
  asm volatile("s_waitcnt vmcnt(0)" : "+v"(v) :: "memory");
  if (!__all(v >= want)) wait_words(p, want);
}
__device__ __forceinline__ void wait_words(GAS unsigned* p, unsigned want) {
  for (unsigned spins = 0; spins < SPIN_CAP; ++spins) {
    if (__all(ld_flag(p) >= want)) break;
    __builtin_amdgcn_s_sleep(2);
  }
}
__device__ __forceinline__ f32x4 phi4(const f32x4& t) {
  const f32x4 a = t * 1.4426950408889634f, b = t + 1.0f;
  f32x4 r;
#pragma unroll
  for (int i = 0; i < 4; ++i) r[i] = t[i] > 0.f ? b[i] : __builtin_amdgcn_exp2f(a[i]);
  return r + 1e-6f;
}
__device__ __forceinline__ f32x2 gelu2(f32x2 u) {
  const float k0 = -2.0f * 0.7978845608028654f * 1.4426950408889634f, k1 = k0 * 0.044715f;
  const f32x2 z = u * ((u * u) * k1 + k0);
  const f32x2 d = (f32x2){__builtin_amdgcn_exp2f(z[0]), __builtin_amdgcn_exp2f(z[1])} + 1.0f;
  return u * (f32x2){__builtin_amdgcn_rcpf(d[0]), __builtin_amdgcn_rcpf(d[1])};
}
__device__ __forceinline__ unsigned gelu_pk(f32x2 c, unsigned bias2) {
  const f32x2 y = gelu2(c + (f32x2){__uint_as_float(bias2 << 16), __uint_as_float(bias2 & 0xffff0000u)});
  return pack2(y[0], y[1]);
}
#define STAMP_DECL
#define STAMP(idx)
#define STAMP_FLUSH(wsp, k)

template <int C> __device__ __forceinline__ float dppf(float v) { return __int_as_float(__builtin_amdgcn_update_dpp(0, __float_as_int(v), C, 0xf, 0xf, true)); }
__device__ __forceinline__ float wave_sum(float v) {
  v += dppf<0xB1>(v); v += dppf<0x4E>(v); v += dppf<0x141>(v); v += dppf<0x140>(v);
  const auto r = __builtin_amdgcn_permlane16_swap(__float_as_uint(v), __float_as_uint(v), false, false);
  v = __uint_as_float(r[0]) + __uint_as_float(r[1]);
  const auto r2 = __builtin_amdgcn_permlane32_swap(__float_as_uint(v), __float_as_uint(v), false, false);
  return __uint_as_float(r2[0]) + __uint_as_float(r2[1]);
}
__device__ __forceinline__ void ln9(float (&v)[9]) {
  float s = 0.f;
#pragma unroll
  for (int j = 0; j < 9; ++j) s += v[j];
  const float mean = wave_sum(s) * (1.0f / D_);
  float q = 0.f;
#pragma unroll
  for (int j = 0; j < 9; ++j) { const float d = v[j] - mean; q += d * d; }
  q = wave_sum(q);
  const float mb = rbf(mean), vb = rbf(q * (1.0f / D_));
  const float rden = __builtin_amdgcn_rcpf(rbf(__builtin_amdgcn_sqrtf(rbf(vb + 1e-5f))));
#pragma unroll
  for (int j = 0; j < 9; ++j) v[j] = rbf(rbf(v[j] - mb) * rden);
}
__global__ __launch_bounds__(512) void k0_prep(const u16* __restrict__ x, const u16* __restrict__ wqkv, const u16* __restrict__ wo, char* __restrict__ ws) {
  constexpr int RS = 1168;
  __shared__ __attribute__((aligned(16))) char sm[2 * 8 * RS];
  const int tid = threadIdx.x, lane = tid & 63, wv = tid >> 6;
  const int q = (lane & 15) >> 2, p = lane & 3;
  if (blockIdx.x < 296) {
    const int b = blockIdx.x;
    const u16* src; int ld, nrows; char* dst;
    if (b < 216) { const int cg = b / 18, ks = b % 18; src = wqkv + (size_t)(32 * ks) * 1728 + 144 * cg; ld = 1728; nrows = 32; dst = ws + WS_WQ + (size_t)b * 9216; }
    else { const int u = b - 216, jd = u / 5, m = u % 5, j = jd >> 2, dt = jd & 3; src = wo + (size_t)(144 * j + 32 * m) * D_ + 144 * dt; ld = D_; nrows = m < 4 ? 32 : 16; dst = ws + WS_WO + (size_t)u * 9216; }
    u32x4 v[2];
#pragma unroll
    for (int i = 0; i < 2; ++i) { const int c = tid + 512 * i, row = c / 18, ch = c % 18; v[i] = (c < 576 && row < nrows) ? *(const u32x4*)(src + (size_t)row * ld + ch * 8) : (u32x4){0u, 0u, 0u, 0u}; }
#pragma unroll
    for (int i = 0; i < 2; ++i) { const int c = tid + 512 * i; if (c < 576) *(u32x4*)(sm + c * 16) = v[i]; }
    __syncthreads();
    const LAS char* t = (const LAS char*)sm + (4 * (lane >> 4) + q) * 288 + 8 * p;
    const rsrc_t rd = mkrsrc(dst, 9216);
    for (int ct = wv; ct < 9; ct += 8) st16(rd, ct * 1024 + lane * 16, cat8(trread(t + ct * 32), trread(t + 4608 + ct * 32)));
    return;
  }
  const int b = blockIdx.x - 296;
  STAMP_DECL;
  STAMP(0);
  if (b == 0 && tid < NCNT) *(unsigned*)(ws + WS_CNT + (size_t)tid * 256) = 0u;
  const int ksi = b >> 2, gw = b & 3;
  const int tok = 32 * ksi + (wv < 4 ? 4 * gw + wv : 16 + 4 * gw + wv - 4);
  const u16* xr = x + (size_t)tok * D_;
  float v[9];
#pragma unroll
  for (int i = 0; i < 9; ++i) v[i] = bf2f(xr[lane + 64 * i]);
  ln9(v);
#pragma unroll
  for (int i = 0; i < 9; ++i) *(__bf16*)(sm + wv * RS + (lane + 64 * i) * 2) = (__bf16)v[i];
  ln9(v);
#pragma unroll
  for (int i = 0; i < 9; ++i) *(__bf16*)(sm + 8 * RS + wv * RS + (lane + 64 * i) * 2) = (__bf16)v[i];
  STAMP(1);
  __syncthreads();
  const rsrc_t rh = mkrsrc(ws + WS_H, 2359296), rx = mkrsrc(ws + WS_X, 2359296);
  {
    const LAS char* hs = (const LAS char*)sm + 8 * RS;
    for (int dt0 = 4 * wv; dt0 < 36; dt0 += 32) {
      const int dt = dt0 + (lane >> 4);
      const bf16x4 lo = trread(hs + q * RS + (16 * dt + 4 * p) * 2), hi = trread(hs + (4 + q) * RS + (16 * dt + 4 * p) * 2);
      st16(rh, ((ksi * 36 + dt) * 64 + gw * 16 + (lane & 15)) * 16, cat8(lo, hi));
    }
  }
  for (int i = tid; i < 576; i += 512) {
    const int rr = i & 7, ch = i >> 3;
    const int kst = ch >> 2, g = ch & 3;
    const int t = 32 * ksi + (rr < 4 ? 4 * gw + rr : 16 + 4 * gw + rr - 4);
    const u32x2 lo = *(const u32x2*)(sm + rr * RS + (32 * kst + 4 * g) * 2), hi = *(const u32x2*)(sm + rr * RS + (32 * kst + 16 + 4 * g) * 2);
    const int rb = t >> 7, st = (t >> 4) & 7, c = t & 15;
    st16(rx, (((rb * 18 + kst) * 8 + st) * 64 + g * 16 + c) * 16, (u32x4){lo[0], lo[1], hi[0], hi[1]});
  }
  STAMP(2);
}

struct FragG { bf16x8 a[9]; bf16x8 w[2]; };
struct FragQ { bf16x8 a[2]; bf16x8 w[9]; };
struct PtrG { const LAS char *pa, *pw0, *pw1; };
struct OffG { unsigned a, w0, w1; };
__device__ __forceinline__ OffG offG(int w, int lane) {
  const int g = lane >> 4, q = (lane & 15) >> 2, p = lane & 3, x = 4 * (g & 1) + q;
  OffG o;
  o.a = lane * 16;
  o.w0 = 18432 + (4 * g + q) * 256 + (((2 * w) ^ x) * 32) + (p >> 1) * 16 + (p & 1) * 8;
  o.w1 = 18432 + (4 * g + q) * 256 + (((2 * w + 1) ^ x) * 32) + (p >> 1) * 16 + (p & 1) * 8;
  return o;
}
__device__ __forceinline__ PtrG ptrG(const LAS char* lds, int slot, const OffG& o) { const LAS char* s = lds + slot * SLOT; return PtrG{s + o.a, s + o.w0, s + o.w1}; }
__device__ __forceinline__ void ldG(FragG& f, const PtrG& P) {
  f.w[0] = cat8(trread(P.pw0), trread(P.pw0 + 4096)); f.w[1] = cat8(trread(P.pw1), trread(P.pw1 + 4096));
#pragma unroll
  for (int a = 0; a < 9; ++a) f.a[a] = ldsb(P.pa + a * 1024);
}
template <bool WROW, int RD, bool LOAD>
__device__ __forceinline__ void stepG(f32x4 (&acc)[2][9], const FragG& c, FragG& n, const PtrG& P, int ks) {
  bf16x4 lo0, hi0, lo1, hi1;
  auto rd = [&](int r) {
    if (!LOAD || r > 12) return;
    if (r == 0) lo0 = trread(P.pw0 + ks * 8192);
    else if (r == 1) { hi0 = trread(P.pw0 + ks * 8192 + 4096); n.w[0] = cat8(lo0, hi0); }
    else if (r == 2) n.a[0] = ldsb(P.pa + ks * 9216);
    else if (r == 3) lo1 = trread(P.pw1 + ks * 8192);
    else if (r == 4) { hi1 = trread(P.pw1 + ks * 8192 + 4096); n.w[1] = cat8(lo1, hi1); }
    else n.a[r - 4] = ldsb(P.pa + ks * 9216 + (r - 4) * 1024);
  };
#pragma unroll
  for (int i = 0; i < 18; ++i) {
    const int a = i >> 1, w = i & 1;
    if (WROW) MFMA_IP(acc[w][a], c.w[w], c.a[a]); else MFMA_IP(acc[w][a], c.a[a], c.w[w]);
    if (RD == 0) rd(i);
    else { if (i < 3) { rd(2 * i); rd(2 * i + 1); } else rd(i + 3); }
    SB();
  }
}
template <bool WROW>
__device__ __forceinline__ void runG(f32x4 (&acc)[2][9], FragG& f0, int& slot, const LAS char* lds, const OffG& o, int n) {
  PtrG P = ptrG(lds, slot, o);
#pragma unroll 1
  for (int t = 0; t < n; ++t) {
    FragG f1;
    stepG<WROW, 1, true>(acc, f0, f1, P, 1);
    WAIT_LGKM0();
    slot = slot + 1 == NS ? 0 : slot + 1;
    P = ptrG(lds, slot, o);
    BAR();
    SB();
    stepG<WROW, 0, true>(acc, f1, f0, P, 0);
  }
}
__device__ __forceinline__ void acc_settle(f32x4 (&acc)[2][9]) {
  asm volatile("s_nop 7\n\ts_nop 7" ::: "memory");
#pragma unroll
  for (int n = 0; n < 2; ++n)
#pragma unroll
    for (int a = 0; a < 9; ++a) asm volatile("" : "+v"(acc[n][a]));
}
__device__ __forceinline__ void acc_zero(f32x4 (&acc)[2][9]) {
#pragma unroll
  for (int n = 0; n < 2; ++n)
#pragma unroll
    for (int a = 0; a < 9; ++a) { acc[n][a] = (f32x4){0.f, 0.f, 0.f, 0.f}; asm volatile("" : "+v"(acc[n][a])); }
  asm volatile("s_nop 1");
}
template <bool WROW, bool LOAD>
__device__ __forceinline__ void stepQ(f32x4 (&acc)[2][9], const FragQ& c, FragQ& n, const LAS char* pa, const LAS char* pw, int ks) {
#pragma unroll
  for (int i = 0; i < 18; ++i) {
    const int k = i >> 1, s = i & 1;
    if (WROW) MFMA_IP(acc[s][k], c.w[k], c.a[s]); else MFMA_IP(acc[s][k], c.a[s], c.w[k]);
    if (LOAD) {
      if (i == 0) n.a[0] = ldsb(pa + ks * 8192);
      else if (i == 1) n.w[0] = ldsb(pw + ks * 9216);
      else if (i == 2) n.a[1] = ldsb(pa + ks * 8192 + 1024);
      else if (i <= 10) n.w[i - 2] = ldsb(pw + ks * 9216 + (i - 2) * 1024);
    }
    SB();
  }
}
__device__ __forceinline__ void ldQ(FragQ& f, const LAS char* pa, const LAS char* pw) {
#pragma unroll
  for (int n = 0; n < 2; ++n) f.a[n] = ldsb(pa + n * 1024);
#pragma unroll
  for (int i = 0; i < 9; ++i) f.w[i] = ldsb(pw + i * 1024);
}
template <bool WROW>
__device__ __forceinline__ void runQ(f32x4 (&acc)[2][9], FragQ& f0, int& slot, const LAS char* lds, unsigned offA, unsigned offW, int n) {
  const LAS char* s = lds + slot * SLOT;
#pragma unroll 1
  for (int t = 0; t < n; ++t) {
    FragQ f1;
    stepQ<WROW, true>(acc, f0, f1, s + offA, s + offW, 1);
    WAIT_LGKM0();
    slot = slot + 1 == NS ? 0 : slot + 1;
    s = lds + slot * SLOT;
    BAR();
    SB();
    stepQ<WROW, true>(acc, f1, f0, s + offA, s + offW, 0);
  }
}
template <bool WROW>
__device__ __forceinline__ void lastQ(f32x4 (&acc)[2][9], FragQ& f0, int slot, const LAS char* lds, unsigned offA, unsigned offW) {
  const LAS char* s = lds + slot * SLOT;
  FragQ f1;
  stepQ<WROW, true>(acc, f0, f1, s + offA, s + offW, 1);
  WAIT_LGKM0();
  SB();
  stepQ<WROW, false>(acc, f1, f0, s + offA, s + offW, 0);
}

template <int NA>
struct LoadG {
  unsigned va[NA], vw[4]; unsigned la, lw; rsrc_t ra, rw; unsigned astep, wstep;
  __device__ __forceinline__ void init(int l, int lane, unsigned ldsbase, rsrc_t ra_, rsrc_t rw_, unsigned ldw, unsigned astep_, unsigned wstep_) {
    ra = ra_; rw = rw_; astep = astep_; wstep = wstep_;
#pragma unroll
    for (int i = 0; i < NA; ++i) { const int a = i < 4 ? l + 4 * i : 16 + l; va[i] = (a / 9) * 36864 + (a % 9) * 1024 + lane * 16; }
#pragma unroll
    for (int i = 0; i < 4; ++i) { const int row = 4 * (l + 4 * i) + (lane >> 4), cp = lane & 15; vw[i] = row * ldw + ((cp ^ ((row & 7) << 1)) * 16); }
    la = ldsbase + l * 1024; lw = ldsbase + 18432 + l * 1024;
  }
  __device__ __forceinline__ void issueW(int slot, int kb) {
    const unsigned sw = (unsigned)kb * wstep, so = slot * SLOT;
#pragma unroll
    for (int i = 0; i < 4; ++i) dma<false>(lw + so + i * 4096, vw[i], rw, sw);
  }
  template <bool SC1>
  __device__ __forceinline__ void issueA(int slot, int kb) {
    const unsigned sa = (unsigned)kb * astep, so = slot * SLOT;
#pragma unroll
    for (int i = 0; i < NA; ++i) dma<SC1>(la + so + i * 4096, va[i], ra, sa);
  }
};
template <int NW>
struct LoadQ {
  unsigned vo, la, lw;
  __device__ __forceinline__ void init(int l, int lane, unsigned ldsbase) { vo = l * 1024 + lane * 16; la = ldsbase + l * 1024; lw = ldsbase + 16384 + l * 1024; }
  __device__ __forceinline__ void issue(int slot, rsrc_t ra, unsigned sa, rsrc_t rw, unsigned sw) {
    const unsigned so = slot * SLOT;
#pragma unroll
    for (int i = 0; i < 4; ++i) dma<false>(la + so + i * 4096, vo, ra, sa + i * 4096);
#pragma unroll
    for (int i = 0; i < NW; ++i) dma<false>(lw + so + i * 4096, vo, rw, sw + i * 4096);
  }
};

struct Args {
  const u16 *x, *wqkv, *wo, *w1, *b1, *w2, *b2;
  u16* out;
  char* ws;
};
constexpr int KWIN = 4;

template <int NA>
__device__ __forceinline__ void p1_load_g(const Args& A, int l, int lane, unsigned ldsbase, int ft, int dtile, int rot) {
  LoadG<NA> L;
  L.init(l, lane, ldsbase, mkrsrc(A.ws + WS_H + dtile * 9216, 2359296 - dtile * 9216), mkrsrc(A.w1 + ft * 128, (unsigned)(S_ * F_ * 2 - ft * 256)), F_ * 2, 73728, 64 * F_ * 2);
  constexpr int NB = 32, N = NA + 4;
  auto kbof = [&](int t) { return (t & ~(KWIN - 1)) | ((t + rot) & (KWIN - 1)); };
  auto iss = [&](int slot, int t) { const int kb = kbof(t); L.issueW(slot, kb); L.template issueA<false>(slot, kb); };
  iss(0, 0); iss(1, 1);
  WAIT_VM(N); BAR();
  iss(2, 2); iss(3, 3);
  int slot = 0;
#pragma unroll 1
  for (int t = 1; t < NB - (NS - 1); ++t) {
    WAIT_VM(N * (NS - 2));
    BAR();
    iss(slot, t + NS - 1);
    slot = slot + 1 == NS ? 0 : slot + 1;
  }
  WAIT_VM(N * 2); BAR();
  WAIT_VM(N * 1); BAR();
  WAIT_VM(0); BAR();
}
template <int NW>
__device__ __forceinline__ void p1_load_q(const Args& A, int l, int lane, unsigned ldsbase, int rb, int cg0, int cg1, int rot) {
  LoadQ<NW> L;
  L.init(l, lane, ldsbase);
  const rsrc_t ra = mkrsrc(A.ws + WS_X, 2359296), rw = mkrsrc(A.ws + WS_WQ, 12 * 18 * 9216);
  constexpr int NB = 18, N = NW + 4;
  auto iss = [&](int slot, int t) {
    const int seg = t >= 9; int kb = (seg ? t - 9 : t) + rot; kb = kb >= 9 ? kb - 9 : kb;
    L.issue(slot, ra, (unsigned)((rb * 9 + kb) * 16384), rw, (unsigned)(((seg ? cg1 : cg0) * 9 + kb) * 18432));
  };
  iss(0, 0); iss(1, 1);
  WAIT_VM(N); BAR();
  iss(2, 2); iss(3, 3);
  int slot = 0;
#pragma unroll 1
  for (int t = 1; t < NB - (NS - 1); ++t) {
    WAIT_VM(N * (NS - 2));
    BAR();
    iss(slot, t + NS - 1);
    slot = slot + 1 == NS ? 0 : slot + 1;
  }
  WAIT_VM(N * 2); BAR();
  WAIT_VM(N * 1); BAR();
  WAIT_VM(0); BAR();
}
template <int NA>
__device__ __forceinline__ void p2_prefetch_w(const Args& A, int l, int lane, unsigned ldsbase, int st, int j, int rot) {
  LoadG<NA> L;
  L.init(l, lane, ldsbase, mkrsrc(A.ws, 0), mkrsrc(A.w2 + (size_t)j * 1152 * S_ + st * 128, (unsigned)(1152 * S_ * 2 - st * 256)), S_ * 2, 0, 64 * S_ * 2);
#pragma unroll
  for (int t = 0; t < NS - 1; ++t) { int k = t + rot; k = k >= 18 ? k - 18 : k; L.issueW(t, k); }
}
template <int NA>
__device__ __forceinline__ void p2_load(const Args& A, int l, int lane, unsigned ldsbase, int st, int dt, int j, int rot) {
  constexpr int NB = 18, N = NA + 4;
  LoadG<NA> L;
  L.init(l, lane, ldsbase, mkrsrc(A.ws + WS_U + (size_t)j * 18 * 73728 + dt * 9216, 18 * 73728 - dt * 9216),
         mkrsrc(A.w2 + (size_t)j * 1152 * S_ + st * 128, (unsigned)(1152 * S_ * 2 - st * 256)), S_ * 2, 73728, 64 * S_ * 2);
  auto kbof = [&](int t) { int k = t + rot; return k >= NB ? k - NB : k; };
  L.template issueA<true>(0, kbof(0)); L.template issueA<true>(1, kbof(1));
  WAIT_VM(NA); BAR();
  L.template issueA<true>(2, kbof(2));
  L.issueW(3, kbof(3)); L.template issueA<true>(3, kbof(3));
  WAIT_VM(NA + N); BAR();
  L.issueW(0, kbof(4)); L.template issueA<true>(0, kbof(4));
  int slot = 1;
#pragma unroll 1
  for (int t = 2; t < NB - 3; ++t) {
    WAIT_VM(2 * N); BAR();
    L.issueW(slot, kbof(t + 3)); L.template issueA<true>(slot, kbof(t + 3));
    slot = slot + 1 == NS ? 0 : slot + 1;
  }
  const rsrc_t ro = mkrsrc(A.ws + WS_O + (size_t)(j * 128 + st * 8) * 5120, 8 * 5120), rw = mkrsrc(A.ws + WS_WO + (size_t)(j * 4 + dt) * 46080, 46080);
  const unsigned vo = lane * 16;
#pragma unroll
  for (int e = 0; e < 3; ++e) {
    WAIT_VM(2 * N); BAR();
    const unsigned so = ldsbase + slot * SLOT;
#pragma unroll
    for (int i = 0; i < 4; ++i) { const int pc = l + 4 * i, ks = pc >> 3, s = pc & 7, m = e < 2 ? 2 * e + ks : 4; dma<true>(so + pc * 1024, vo, ro, (s * 5 + m) * 1024); }
#pragma unroll
    for (int i = 0; i < NA; ++i) { const int pc = i < 4 ? l + 4 * i : 16 + l, ks = pc >= 9, a = pc - 9 * ks, m = e < 2 ? 2 * e + ks : 4; dma<false>(so + 16384 + pc * 1024, vo, rw, (m * 9 + a) * 1024); }
    slot = slot + 1 == NS ? 0 : slot + 1;
  }
  WAIT_VM(N * 2); BAR();
  WAIT_VM(N * 1); BAR();
  WAIT_VM(0); BAR();
}

__device__ __forceinline__ void o_tile(f32x4 (&o)[2], bool two, const bf16x8& k0, const bf16x8& z0, const bf16x8 (&p0)[2], const bf16x8& k1, const bf16x8& z1, const bf16x8 (&p1)[2]) {
#pragma unroll
  for (int n = 0; n < 2; ++n) {
    f32x4 num = MFMA(k1, p1[n], ((f32x4){0.f, 0.f, 0.f, 0.f})), den = MFMA(z1, p1[n], ((f32x4){0.f, 0.f, 0.f, 0.f}));
    if (two) { num = MFMA(k0, p0[n], num); den = MFMA(z0, p0[n], den); }
#pragma unroll
    for (int r = 0; r < 4; ++r) o[n][r] = num[r] * __builtin_amdgcn_rcpf(den[r] + 1e-6f);
  }
}

__global__ __launch_bounds__(512, 2) void k1_main(Args A) {
  __shared__ __attribute__((aligned(16))) char smem[LDS_BYTES];
  const LAS char* lds = (const LAS char*)smem;
  const unsigned ldsbase = (unsigned)(size_t)lds;
  const int tid = threadIdx.x, lane = tid & 63, wv = __builtin_amdgcn_readfirstlane(tid >> 6);
  const int b = blockIdx.x, xcd = b & 7, i = b >> 3;
  const int g = lane >> 4;
  const int j2 = xcd & 3, stl = i >> 2, st = 8 * (xcd >> 2) + stl, dt = i & 3;
  const int rot2 = ((9 * stl + 18 * dt) >> 2) % 18;
  const int nG = xcd < 4 ? 20 : 16;
  STAMP_DECL;
  STAMP(0);
  if (i < nG) {
    const int ft = (xcd < 4 ? 5 * xcd : 20 + 4 * (xcd - 4)) + (i >> 2), dtile = i & 3;
    const int rot = dtile + (KWIN * (i >> 2)) / (xcd < 4 ? 5 : 4);
    const int w4 = wv & 3;
    const int f0 = ft * 128 + 32 * w4 + 4 * g;
    const u32x2 bl = *(const u32x2*)(A.b1 + f0), bh = *(const u32x2*)(A.b1 + f0 + 16);
    const rsrc_t ru = mkrsrc(A.ws + WS_U + ((size_t)(ft * 4 + w4) * 36 + dtile * 9) * 1024, 9216);
    LAS f32x4* xfer = (LAS f32x4*)(smem + 3 * SLOT) + w4 * 512 + lane;
    auto gelu_store = [&](int a, const f32x4& c0, const f32x4& c1) {
      st16(ru, a * 1024 + lane * 16, (u32x4){gelu_pk((f32x2){c0[0], c0[1]}, bl[0]), gelu_pk((f32x2){c0[2], c0[3]}, bl[1]), gelu_pk((f32x2){c1[0], c1[1]}, bh[0]), gelu_pk((f32x2){c1[2], c1[3]}, bh[1])});
    };
    if (wv >= 4) {
      if (wv < 6) { p1_load_g<5>(A, wv - 4, lane, ldsbase, ft, dtile, rot); p2_prefetch_w<5>(A, wv - 4, lane, ldsbase, st, j2, rot2); }
      else { p1_load_g<4>(A, wv - 4, lane, ldsbase, ft, dtile, rot); p2_prefetch_w<4>(A, wv - 4, lane, ldsbase, st, j2, rot2); }
      BAR();
      BAR();
#pragma unroll
      for (int a = 0; a < 4; ++a) { const f32x4 c0 = xfer[(2 * a) * 64], c1 = xfer[(2 * a + 1) * 64]; gelu_store(5 + a, c0, c1); }
    } else {
      f32x4 acc[2][9];
      acc_zero(acc);
      const OffG o = offG(wv, lane);
      FragG fa; int slot = 0;
      BAR();
      ldG(fa, ptrG(lds, 0, o));
      SB();
      STAMP(1);
      runG<true>(acc, fa, slot, lds, o, 31);
      { FragG fb; const PtrG P = ptrG(lds, slot, o);
        stepG<true, 1, true>(acc, fa, fb, P, 1); WAIT_LGKM0(); SB(); stepG<true, 0, false>(acc, fb, fa, P, 0); }
      acc_settle(acc);
      STAMP(2);
      BAR();
#pragma unroll
      for (int a = 0; a < 4; ++a) { xfer[(2 * a) * 64] = acc[0][5 + a]; xfer[(2 * a + 1) * 64] = acc[1][5 + a]; }
      WAIT_LGKM0();
      BAR();
#pragma unroll
      for (int a = 0; a < 5; ++a) gelu_store(a, acc[0][a], acc[1][a]);
    }
    WAIT_VM(0);
    BAR();
    if (tid == 0) st_flag(cntp(A.ws, C_UF + ft * 4 + dtile));
    STAMP(3);
  } else {
    const int u = i - nG, pr = xcd & 1;
    const unsigned offA = 2 * (wv & 3) * 1024 + lane * 16, offW = 16384 + lane * 16;
    if (u >= 12) {
      const int sp = (xcd - 4) * 4 + (u - 12);
      if (wv >= 4) { if (wv < 6) p2_prefetch_w<5>(A, wv - 4, lane, ldsbase, st, j2, rot2); else p2_prefetch_w<4>(A, wv - 4, lane, ldsbase, st, j2, rot2); }
      const rsrc_t rs = mkrsrc(A.ws + WS_XCH + (size_t)sp * (4 * 16 * 9216), 4 * 16 * 9216);
      for (int e = tid; e < 4 * 16 * 576; e += 512) st16(rs, e * 16, (u32x4){~0u, ~0u, ~0u, ~0u});
      WAIT_VM(0);
      BAR();
      if (tid == 0) add_cnt(cntp(A.ws, C_SR));
      const int j = sp & 3, part = sp >> 2;
      if (wv == 0) wait_words(cntp(A.ws, C_KVC + j), 16u);
      BAR();
      STAMP(1);
      float* tot = (float*)smem;
      {
        const int h0 = part == 0 ? 0 : (part == 1 ? 1 : 4), c0 = (h0 * 324) >> 3;
        const int ch = tid < 164 ? c0 + tid : 324 + (tid - 164);
        if (tid < 182) {
          const rsrc_t rp = mkrsrc(A.ws + WS_KVP, 64 * 5472);
          u32x4 v[16];
#pragma unroll
          for (int r = 0; r < 16; ++r) v[r] = ld16(rp, (unsigned)((r * 4 + j) * 5472 + ch * 16));
          f32x4 lo = {0.f, 0.f, 0.f, 0.f}, hi = {0.f, 0.f, 0.f, 0.f};
#pragma unroll
          for (int r = 0; r < 16; ++r) { lo += unpk_lo(v[r]); hi += unpk_hi(v[r]); }
          *(f32x4*)(tot + ch * 8) = lo; *(f32x4*)(tot + ch * 8 + 4) = hi;
        }
      }
      __syncthreads();
      STAMP(2);
      const rsrc_t rz = mkrsrc(A.ws + WS_KVZ + (size_t)j * 34816, 34816);
#pragma unroll
      for (int k = 0; k < 2; ++k) {
        const int fi = part * 9 + (k ? 8 : wv);
        if (fi < 34 && (k == 0 || wv == 0)) {
          const int pi = fi >> 1, kind = fi & 1;
          const int ii = (pi + 1) >> 1, m = (ii == 0 ? 0 : (ii - 1) >> 1) + (pi > 0 && !(pi & 1) ? 1 : 0);
          const int cp = 16 * ii + (lane & 15), hp = (cp * 3641) >> 16;
          const int c0 = 32 * m + 4 * g, mul = kind ? 1 : 18;
          const int base = kind ? 2592 + c0 : 18 * (c0 - hp) + cp, rel = c0 - 18 * hp;
          float val[8];
#pragma unroll
          for (int jj = 0; jj < 8; ++jj) {
            const int o = jj < 4 ? jj : 12 + jj;
            const bool ok = (unsigned)(rel + o) < 18u;
            const float v = tot[ok ? base + o * mul : 0];
            val[jj] = ok ? v : 0.f;
          }
          st16(rz, fi * 1024 + lane * 16, (u32x4){pack2(val[0], val[1]), pack2(val[2], val[3]), pack2(val[4], val[5]), pack2(val[6], val[7])});
        }
      }
      WAIT_VM(0);
      BAR();
      if (tid == 0) add_cnt(cntp(A.ws, C_KF + j));
      STAMP(3);
    } else if (u >= 8) {
      const int rb = 4 * (xcd >> 1) + (u - 8), j0 = 2 * pr;
      const int rot = (9 * (u - 8)) / 4 + 1;
      if (wv >= 4) {
        if (wv < 6) p1_load_q<5>(A, wv - 4, lane, ldsbase, rb, j0, j0 + 1, rot); else p1_load_q<4>(A, wv - 4, lane, ldsbase, rb, j0, j0 + 1, rot);
      } else {
        FragQ f0; f32x4 acc[2][9]; int slot = 0;
        auto epi = [&](int j) {
#pragma unroll
          for (int n = 0; n < 2; ++n) {
            const rsrc_t P = mkrsrc(A.ws + WS_PQ + ((size_t)(j * 128 + rb * 8 + 2 * wv + n) * 5) * 1024, 5120);
#pragma unroll
            for (int m = 0; m < 5; ++m) {
              const f32x4 p0 = phi4(acc[n][2 * m]), p1 = m < 4 ? phi4(acc[n][m < 4 ? 2 * m + 1 : 0]) : (f32x4){0.f, 0.f, 0.f, 0.f};
              st16(P, m * 1024 + lane * 16, pack8(p0, p1));
            }
          }
        };
        BAR();
        ldQ(f0, lds + offA, lds + offW);
        acc_zero(acc);
        SB();
        runQ<true>(acc, f0, slot, lds, offA, offW, 9);
        acc_settle(acc);
        epi(j0);
        acc_zero(acc);
        SB();
        runQ<true>(acc, f0, slot, lds, offA, offW, 8);
        lastQ<true>(acc, f0, slot, lds, offA, offW);
        acc_settle(acc);
        epi(j0 + 1);
      }
      WAIT_VM(0);
      BAR();
      if (tid == 0) st_flag(cntp(A.ws, C_PQF + rb * 2 + pr));
      STAMP(3);
    } else {
      const int rb = 4 * (xcd >> 1) + (u >> 1), j = 2 * pr + (u & 1);
      const int rot = (9 * (u >> 1)) / 4 + 3 * (u & 1);
      LAS char* fr = (LAS char*)smem;
      float* cmp = (float*)(smem + XTRA);
      if (wv >= 4) {
        if (wv < 6) p1_load_q<5>(A, wv - 4, lane, ldsbase, rb, 4 + j, 8 + j, rot); else p1_load_q<4>(A, wv - 4, lane, ldsbase, rb, 4 + j, 8 + j, rot);
        BAR();
      } else {
        bf16x8 pk[9];
        FragQ f0; f32x4 acc[2][9]; int slot = 0;
        BAR();
        ldQ(f0, lds + offA, lds + offW);
        acc_zero(acc);
        SB();
        runQ<false>(acc, f0, slot, lds, offA, offW, 9);
        acc_settle(acc);
#pragma unroll
        for (int c = 0; c < 9; ++c) pk[c] = pack8(phi4(acc[0][c]), phi4(acc[1][c]));
        acc_zero(acc);
        SB();
        runQ<false>(acc, f0, slot, lds, offA, offW, 8);
        lastQ<false>(acc, f0, slot, lds, offA, offW);
        acc_settle(acc);
        STAMP(1);
        BAR();
#pragma unroll
        for (int c = 0; c < 9; ++c) {
          *(LAS bf16x8*)(fr + (c * 4 + wv) * 1024 + lane * 16) = pk[c];
          *(LAS bf16x8*)(fr + ((9 + c) * 4 + wv) * 1024 + lane * 16) = pack8(acc[0][c], acc[1][c]);
        }
        if (wv == 0) *(LAS u32x4*)(fr + 73728 + lane * 16) = (u32x4){0x3f803f80u, 0x3f803f80u, 0x3f803f80u, 0x3f803f80u};
      }
      __syncthreads();
      {
        const int cl = lane & 15;
        f32x4 d[5];
        int ja[5], jc[5];
#pragma unroll
        for (int k = 0; k < 5; ++k) {
          const int jb0 = wv + 8 * k, jb = jb0 < 34 ? jb0 : 33;
          const int isz = jb >= 25, t = jb < 2 ? 0 : jb - 2, t3 = (t * 86) >> 8;
          ja[k] = isz ? jb - 25 : (jb < 2 ? 0 : 1 + t3);
          jc[k] = isz ? -1 : (jb < 2 ? jb : t3 + t - 3 * t3);
        }
        bf16x8 fa[2][4], fb[2][4];
        auto ldjob = [&](int k, int buf) {
          const LAS char* pa = fr + ja[k] * 4096 + lane * 16;
          const LAS char* pb = fr + (jc[k] < 0 ? 73728 : (9 + jc[k]) * 4096) + lane * 16;
          const int sb = jc[k] < 0 ? 0 : 1024;
#pragma unroll
          for (int r = 0; r < 4; ++r) { fa[buf][r] = ldsb(pa + r * 1024); fb[buf][r] = ldsb(pb + r * sb); }
        };
        ldjob(0, 0);
#pragma unroll
        for (int k = 0; k < 5; ++k) {
          if (k < 4) ldjob(k + 1, (k + 1) & 1);
          d[k] = (f32x4){0.f, 0.f, 0.f, 0.f};
#pragma unroll
          for (int r = 0; r < 4; ++r) d[k] = MFMA(fa[k & 1][r], fb[k & 1][r], d[k]);
        }
        LAS float* cm = (LAS float*)(smem + XTRA);
#pragma unroll
        for (int k = 0; k < 5; ++k) {
          const bool isz = jc[k] < 0;
          const int row0 = 16 * ja[k] + 4 * g, col = 16 * jc[k] + cl, hc = (col * 3641) >> 16;
          const int lo = isz ? (cl == 0 ? row0 - 8 : 4096) : 18 * hc;
          const int idx = isz ? 2592 + row0 : 18 * (row0 - hc) + col, step = isz ? 1 : 18;
#pragma unroll
          for (int r = 0; r < 4; ++r) cm[(unsigned)(row0 + r - lo) < 18u ? idx + r * step : 2736 + lane] = d[k][r];
        }
      }
      __syncthreads();
      const rsrc_t rp = mkrsrc(A.ws + WS_KVP, 64 * 5472);
      if (tid < 342) st16(rp, (unsigned)((rb * 4 + j) * 5472 + tid * 16), pack8(*(const f32x4*)(cmp + tid * 8), *(const f32x4*)(cmp + tid * 8 + 4)));
      WAIT_VM(0);
      BAR();
      if (tid == 0) add_cnt(cntp(A.ws, C_KVC + j));
      STAMP(3);
    }
    __syncthreads();
    if (wv >= 4 && u < 12) { if (wv < 6) p2_prefetch_w<5>(A, wv - 4, lane, ldsbase, st, j2, rot2); else p2_prefetch_w<4>(A, wv - 4, lane, ldsbase, st, j2, rot2); }
  }
  const int T = st * 4 + dt;
  const rsrc_t rx = mkrsrc(A.ws + WS_XCH, 64u * 16u * 9216u);
  LAS f32x4* own = (LAS f32x4*)smem;
  const int na = wv == 0 ? 2 : 1;
  u32x2 xr[2][2]; float b2v[2];
  auto ld_resid = [&]() {
#pragma unroll
    for (int n = 0; n < 2; ++n) {
      const int s = st * 128 + 32 * j2 + 16 * n + (lane & 15);
      b2v[n] = bf2f(A.b2[s]);
#pragma unroll
      for (int k = 0; k < 2; ++k) xr[k][n] = *(const u32x2*)(A.x + (size_t)s * D_ + dt * 144 + 16 * (k ? 8 : wv) + 4 * g);
    }
  };
  if (wv == 4) { int k = rot2 + (lane < 3 ? lane : 0); k = k >= 18 ? k - 18 : k; wait_words(cntp(A.ws, C_UF + (9 * j2 + (k >> 1)) * 4 + dt), 1u); }
  BAR();
  STAMP(4);
  if (wv >= 4) {
    if (wv < 6) p2_load<5>(A, wv - 4, lane, ldsbase, st, dt, j2, rot2); else p2_load<4>(A, wv - 4, lane, ldsbase, st, dt, j2, rot2);
    ld_resid();
    BAR();
  } else {
    f32x4 acc[2][9];
    acc_zero(acc);
    const OffG o = offG(wv, lane);
    FragG fa; int slot = 0;
    if (wv == 0) wait_words(cntp(A.ws, C_UF + (9 * j2 + (lane < 9 ? lane : 0)) * 4 + dt), 1u);
    BAR();
    ldG(fa, ptrG(lds, 0, o));
    SB();
    STAMP(5);
    GAS unsigned* wp1 = lane == 0 ? cntp(A.ws, C_KF + j2) : cntp(A.ws, C_PQF + st * 2 + (j2 >> 1));
    runG<false>(acc, fa, slot, lds, o, 2);
    unsigned pv1 = 0;
    if (wv == 0) pv1 = poll_issue(wp1);
    runG<false>(acc, fa, slot, lds, o, 1);
    if (wv == 0) poll_check(pv1, wp1, lane == 0 ? 4u : 1u);
    runG<false>(acc, fa, slot, lds, o, 1);
    STAMP(10);
    {
      const rsrc_t rk = mkrsrc(A.ws + WS_KVZ + (size_t)j2 * 34816, 34816), rpq = mkrsrc(A.ws + WS_PQ + (size_t)(j2 * 128 + st * 8 + 2 * dt) * 5120, 2 * 5120);
      const rsrc_t ro = mkrsrc(A.ws + WS_O + (size_t)(j2 * 128 + st * 8 + 2 * dt) * 5120, 2 * 5120);
      const unsigned vo = lane * 16;
      const int m = wv, pa = m == 0 ? 0 : 4 * m - 1;
      bf16x8 kz[8], pq[3][2];
#pragma unroll
      for (int e = 0; e < 4; ++e) { const int pi = e == 0 ? pa : 4 * m + e - 1; kz[2 * e] = ld16b(rk, vo + (2 * pi) * 1024); kz[2 * e + 1] = ld16b(rk, vo + (2 * pi + 1) * 1024); }
#pragma unroll
      for (int e = 0; e < 3; ++e) { const int ks = m + e - 1 < 0 ? 0 : m + e - 1;
#pragma unroll
        for (int n = 0; n < 2; ++n) pq[e][n] = ld16b(rpq, vo + (n * 5 + ks) * 1024); }
      SB();
      runG<false>(acc, fa, slot, lds, o, 2);
      f32x4 oa[2], ob[2];
      o_tile(oa, m > 0, kz[0], kz[1], pq[0], kz[2], kz[3], pq[1]);
      o_tile(ob, true, kz[4], kz[5], pq[1], kz[6], kz[7], pq[2]);
#pragma unroll
      for (int n = 0; n < 2; ++n) st16(ro, vo + (n * 5 + m) * 1024, pack8(oa[n], ob[n]));
      bf16x8 k8[4], p8[2][2];
      if (m == 0) {
#pragma unroll
        for (int e = 0; e < 4; ++e) k8[e] = ld16b(rk, vo + (30 + e) * 1024);
#pragma unroll
        for (int e = 0; e < 2; ++e)
#pragma unroll
          for (int n = 0; n < 2; ++n) p8[e][n] = ld16b(rpq, vo + (n * 5 + 3 + e) * 1024);
      }
      SB();
      runG<false>(acc, fa, slot, lds, o, 2);
      if (m == 0) {
        o_tile(oa, true, k8[0], k8[1], p8[0], k8[2], k8[3], p8[1]);
#pragma unroll
        for (int n = 0; n < 2; ++n) st16(ro, vo + (n * 5 + 4) * 1024, pack8(oa[n], ((f32x4){0.f, 0.f, 0.f, 0.f})));
      }
      SB();
    }
    STAMP(11);
    runG<false>(acc, fa, slot, lds, o, 2);
    WAIT_VM(0);
    if (lane == 0) add_cnt(cntp(A.ws, C_OC + st * 4 + j2));
    GAS unsigned* wp2 = lane == 0 ? cntp(A.ws, C_SR) : cntp(A.ws, C_OC + st * 4 + j2);
    runG<false>(acc, fa, slot, lds, o, 3);
    unsigned pv2 = 0;
    if (wv == 0) pv2 = poll_issue(wp2);
    runG<false>(acc, fa, slot, lds, o, 1);
    if (wv == 0) poll_check(pv2, wp2, 16u);
    runG<false>(acc, fa, slot, lds, o, 3);
    ld_resid();
    const unsigned offA = 2 * wv * 1024 + lane * 16, offW = 16384 + lane * 16;
    FragQ q0;
    { FragG fb; const PtrG P = ptrG(lds, slot, o);
      stepG<false, 1, true>(acc, fa, fb, P, 1);
      WAIT_LGKM0();
      slot = slot + 1 == NS ? 0 : slot + 1;
      BAR();
      ldQ(q0, lds + slot * SLOT + offA, lds + slot * SLOT + offW);
      SB();
      stepG<false, 0, false>(acc, fb, fa, P, 0); }
    runQ<true>(acc, q0, slot, lds, offA, offW, 2);
    { FragQ q1; stepQ<true, false>(acc, q0, q1, lds, lds, 0); }
    acc_settle(acc);
    STAMP(6);
    BAR();
    if (wv != j2) {
      const unsigned base = (unsigned)(((T * 4 + wv) * 4 + j2) * 9216) + lane * 16;
#pragma unroll
      for (int a = 0; a < 9; ++a) st16(rx, base + a * 1024, pack8(acc[0][a], acc[1][a]));
    } else {
#pragma unroll
      for (int n = 0; n < 2; ++n)
#pragma unroll
        for (int a = 0; a < 9; ++a) own[(n * 9 + a) * 64 + lane] = acc[n][a];
      WAIT_LGKM0();
    }
  }
  STAMP(7);
  BAR();
  STAMP(8);
  u32x4 pv[2][3];
  for (unsigned spins = 0; spins < SPIN_CAP; ++spins) {
    bool ok = true;
#pragma unroll
    for (int k = 0; k < 2; ++k)
#pragma unroll
      for (int e = 0; e < 3; ++e) { const int src = e + (e >= j2 ? 1 : 0);
        pv[k][e] = ld16(rx, (k < na) ? (unsigned)(((T * 4 + j2) * 4 + src) * 9216 + (k ? 8 : wv) * 1024) + lane * 16 : 0x80000000u); }
#pragma unroll
    for (int k = 0; k < 2; ++k)
#pragma unroll
      for (int e = 0; e < 3; ++e) ok = ok && pv[k][e][0] != ~0u && pv[k][e][1] != ~0u && pv[k][e][2] != ~0u && pv[k][e][3] != ~0u;
    if (__all(ok)) break;
    __builtin_amdgcn_s_sleep(XSLEEP);
  }
#pragma unroll
  for (int k = 0; k < 2; ++k) {
    if (k < na) {
      const int a = k ? 8 : wv;
#pragma unroll
      for (int n = 0; n < 2; ++n) {
        f32x4 sum = own[(n * 9 + a) * 64 + lane];
#pragma unroll
        for (int e = 0; e < 3; ++e) sum += n ? unpk_hi(pv[k][e]) : unpk_lo(pv[k][e]);
        const int s = st * 128 + 32 * j2 + 16 * n + (lane & 15), d = dt * 144 + 16 * a + 4 * g;
        const unsigned y0 = xr[k][n][0], y1 = xr[k][n][1];
        const float x0 = __uint_as_float(y0 << 16), x1 = __uint_as_float(y0 & 0xffff0000u), x2 = __uint_as_float(y1 << 16), x3 = __uint_as_float(y1 & 0xffff0000u);
        *(u32x2*)(A.out + (size_t)s * D_ + d) = (u32x2){pack2(x0 + (sum[0] + b2v[n]), x1 + (sum[1] + b2v[n])), pack2(x2 + (sum[2] + b2v[n]), x3 + (sum[3] + b2v[n]))};
      }
    }
  }
  STAMP(9);
  STAMP_FLUSH(A.ws, 1);
}

extern "C" void kernel_launch(void* const* d_in, const int* in_sizes, int n_in, void* d_out, int out_size, void* d_ws, size_t ws_size, hipStream_t stream) {
  Args A;
  A.x = (const u16*)d_in[0]; A.wqkv = (const u16*)d_in[1]; A.wo = (const u16*)d_in[2];
  A.w1 = (const u16*)d_in[4]; A.b1 = (const u16*)d_in[5]; A.w2 = (const u16*)d_in[6]; A.b2 = (const u16*)d_in[7];
  A.out = (u16*)d_out; A.ws = (char*)d_ws;
  k0_prep<<<552, 512, 0, stream>>>(A.x, A.wqkv, A.wo, A.ws);
  k1_main<<<256, 512, 0, stream>>>(A);
}
```

```cpp
#include <hip/hip_runtime.h>
#include <hip/hip_bf16.h>

typedef unsigned short u16;
typedef __bf16 bf16x8 __attribute__((ext_vector_type(8)));
typedef __bf16 bf16x4 __attribute__((ext_vector_type(4)));
typedef float f32x4 __attribute__((ext_vector_type(4)));
typedef float f32x2 __attribute__((ext_vector_type(2)));
typedef unsigned u32x4 __attribute__((ext_vector_type(4)));
typedef unsigned u32x2 __attribute__((ext_vector_type(2)));
#define LAS __attribute__((address_space(3)))
#define GAS __attribute__((address_space(1)))
typedef __amdgpu_buffer_rsrc_t rsrc_t;

constexpr int S_ = 2048, D_ = 576, F_ = 4608;
constexpr int NS = 4;
constexpr unsigned SLOT = 34816;
constexpr unsigned LDS_BYTES = 163840;
constexpr unsigned XTRA = NS * SLOT;

constexpr size_t WS_H = 0;
constexpr size_t WS_X = WS_H + 2359296;
constexpr size_t WS_U = WS_X + 2359296;
constexpr size_t WS_PQ = WS_U + 5308416;
constexpr size_t WS_O = WS_PQ + 2621440;
constexpr size_t WS_KVP = WS_O + 2621440;
constexpr size_t WS_KVZ = WS_KVP + 64 * 5472;
constexpr size_t WS_XCH = WS_KVZ + 139264;
constexpr size_t WS_CNT = WS_XCH + 64 * 16 * 9216;
constexpr int C_KVC = 0, C_KF = 4, C_PQF = 8, C_UF = 40, C_OC = 184, C_XC = 248, NCNT = 504;
constexpr unsigned SPIN_CAP = 1u << 15;

__device__ __forceinline__ float bf2f(u16 v) { return __uint_as_float((unsigned)v << 16); }
__device__ __forceinline__ float rbf(float f) { return (float)(__bf16)f; }
__device__ __forceinline__ unsigned pack2(float lo, float hi) {
  typedef __bf16 bf16x2 __attribute__((ext_vector_type(2)));
  bf16x2 v = {(__bf16)lo, (__bf16)hi};
  return __builtin_bit_cast(unsigned, v);
}
__device__ __forceinline__ bf16x8 pack8(const f32x4& a, const f32x4& b) {
  u32x4 w = {pack2(a[0], a[1]), pack2(a[2], a[3]), pack2(b[0], b[1]), pack2(b[2], b[3])};
  return __builtin_bit_cast(bf16x8, w);
}
__device__ __forceinline__ f32x4 unpk_lo(const u32x4& v) { return (f32x4){__uint_as_float(v[0] << 16), __uint_as_float(v[0] & 0xffff0000u), __uint_as_float(v[1] << 16), __uint_as_float(v[1] & 0xffff0000u)}; }
__device__ __forceinline__ f32x4 unpk_hi(const u32x4& v) { return (f32x4){__uint_as_float(v[2] << 16), __uint_as_float(v[2] & 0xffff0000u), __uint_as_float(v[3] << 16), __uint_as_float(v[3] & 0xffff0000u)}; }
__device__ __forceinline__ rsrc_t mkrsrc(const void* p, unsigned bytes) { return __builtin_amdgcn_make_buffer_rsrc((void*)p, (short)0, (int)bytes, 0x00020000); }
__device__ __forceinline__ bf16x4 trread(const LAS char* p) { return __builtin_amdgcn_ds_read_tr16_b64_v4bf16((LAS bf16x4*)p); }
__device__ __forceinline__ bf16x8 cat8(bf16x4 lo, bf16x4 hi) { return __builtin_shufflevector(lo, hi, 0, 1, 2, 3, 4, 5, 6, 7); }
__device__ __forceinline__ bf16x8 ldsb(const LAS char* p) { return *(const LAS bf16x8*)p; }
#define MFMA(a, b, c) __builtin_amdgcn_mfma_f32_16x16x32_bf16((a), (b), (c), 0, 0, 0)
#define MFMA_IP(acc, a, b) asm volatile("v_mfma_f32_16x16x32_bf16 %0, %1, %2, %0" : "+v"(acc) : "v"(a), "v"(b))
#define SB() __builtin_amdgcn_sched_barrier(0)
#define FENCE() asm volatile("" ::: "memory")
#define BAR() do { FENCE(); __builtin_amdgcn_s_barrier(); FENCE(); } while (0)
#define WAIT_LGKM0() asm volatile("s_waitcnt lgkmcnt(0)" ::: "memory")
#define WAIT_VM(n) asm volatile("s_waitcnt vmcnt(%0)" ::"n"(n) : "memory")
template <bool SC1>
__device__ __forceinline__ void dma(unsigned ldsaddr, unsigned voff, rsrc_t r, unsigned soff) {
  if (SC1) asm volatile("s_mov_b32 m0, %0\n\ts_nop 0\n\tbuffer_load_dwordx4 %1, %2, %3 offen sc1 lds" ::"s"(ldsaddr), "v"(voff), "s"(r), "s"(soff) : "memory");
  else asm volatile("s_mov_b32 m0, %0\n\ts_nop 0\n\tbuffer_load_dwordx4 %1, %2, %3 offen lds" ::"s"(ldsaddr), "v"(voff), "s"(r), "s"(soff) : "memory");
}
__device__ __forceinline__ void st16(rsrc_t r, unsigned off, u32x4 v) { __builtin_amdgcn_raw_buffer_store_b128(v, r, off, 0, 16); }
__device__ __forceinline__ void st16(rsrc_t r, unsigned off, bf16x8 v) { __builtin_amdgcn_raw_buffer_store_b128(__builtin_bit_cast(u32x4, v), r, off, 0, 16); }
__device__ __forceinline__ u32x4 ld16(rsrc_t r, unsigned off) { return __builtin_amdgcn_raw_buffer_load_b128(r, off, 0, 16); }
__device__ __forceinline__ GAS unsigned* cntp(char* ws, int i) { return (GAS unsigned*)(ws + WS_CNT + (size_t)i * 256); }
__device__ __forceinline__ unsigned ld_flag(GAS unsigned* p) { return __hip_atomic_load(p, __ATOMIC_RELAXED, __HIP_MEMORY_SCOPE_AGENT); }
__device__ __forceinline__ void st_flag(GAS unsigned* p) { __hip_atomic_store(p, 1u, __ATOMIC_RELAXED, __HIP_MEMORY_SCOPE_AGENT); }
__device__ __forceinline__ unsigned add_cnt(GAS unsigned* p) { return __hip_atomic_fetch_add(p, 1u, __ATOMIC_RELAXED, __HIP_MEMORY_SCOPE_AGENT); }
__device__ __forceinline__ void poll_issue(unsigned& v, GAS unsigned* p) { asm volatile("global_load_dword %0, %1, off sc1" : "+v"(v) : "v"(p) : "memory"); }
__device__ __forceinline__ void wait_words(GAS unsigned* p, unsigned want);
__device__ __forceinline__ void poll_check(unsigned v, GAS unsigned* p, unsigned want) {
  asm volatile("s_waitcnt vmcnt(0)" : "+v"(v) :: "memory");
  if (!__all(v >= want)) wait_words(p, want);
}
__device__ __forceinline__ void wait_words(GAS unsigned* p, unsigned want) {
  for (unsigned spins = 0; spins < SPIN_CAP; ++spins) {
    if (__all(ld_flag(p) >= want)) break;
    __builtin_amdgcn_s_sleep(2);
  }
}
__device__ __forceinline__ f32x4 phi4(const f32x4& t) {
  const f32x4 a = t * 1.4426950408889634f, b = t + 1.0f;
  f32x4 r;
#pragma unroll
  for (int i = 0; i < 4; ++i) r[i] = t[i] > 0.f ? b[i] : __builtin_amdgcn_exp2f(a[i]);
  return r + 1e-6f;
}
__device__ __forceinline__ f32x2 gelu2(f32x2 u) {
  const float k0 = -2.0f * 0.7978845608028654f * 1.4426950408889634f, k1 = k0 * 0.044715f;
  const f32x2 z = u * ((u * u) * k1 + k0);
  const f32x2 d = (f32x2){__builtin_amdgcn_exp2f(z[0]), __builtin_amdgcn_exp2f(z[1])} + 1.0f;
  return u * (f32x2){__builtin_amdgcn_rcpf(d[0]), __builtin_amdgcn_rcpf(d[1])};
}
__device__ __forceinline__ unsigned gelu_pk(f32x2 c, unsigned bias2) {
  const f32x2 y = gelu2(c + (f32x2){__uint_as_float(bias2 << 16), __uint_as_float(bias2 & 0xffff0000u)});
  return pack2(y[0], y[1]);
}
template <int C> __device__ __forceinline__ float dppf(float v) { return __int_as_float(__builtin_amdgcn_update_dpp(0, __float_as_int(v), C, 0xf, 0xf, true)); }
__device__ __forceinline__ float wave_sum(float v) {
  v += dppf<0xB1>(v); v += dppf<0x4E>(v); v += dppf<0x141>(v); v += dppf<0x140>(v);
  const auto r = __builtin_amdgcn_permlane16_swap(__float_as_uint(v), __float_as_uint(v), false, false);
  v = __uint_as_float(r[0]) + __uint_as_float(r[1]);
  const auto r2 = __builtin_amdgcn_permlane32_swap(__float_as_uint(v), __float_as_uint(v), false, false);
  return __uint_as_float(r2[0]) + __uint_as_float(r2[1]);
}
__device__ __forceinline__ void ln9(float (&v)[9]) {
  float s = 0.f;
#pragma unroll
  for (int j = 0; j < 9; ++j) s += v[j];
  const float mean = wave_sum(s) * (1.0f / D_);
  float q = 0.f;
#pragma unroll
  for (int j = 0; j < 9; ++j) { const float d = v[j] - mean; q += d * d; }
  q = wave_sum(q);
  const float mb = rbf(mean), vb = rbf(q * (1.0f / D_));
  const float rden = __builtin_amdgcn_rcpf(rbf(__builtin_amdgcn_sqrtf(rbf(vb + 1e-5f))));
#pragma unroll
  for (int j = 0; j < 9; ++j) v[j] = rbf(rbf(v[j] - mb) * rden);
}
__global__ __launch_bounds__(512) void k0_prep(const u16* __restrict__ x, char* __restrict__ ws) {
  constexpr int RS = 1168;
  __shared__ __attribute__((aligned(16))) char sm[2 * 8 * RS];
  const int tid = threadIdx.x, lane = tid & 63, wv = tid >> 6;
  const int q = (lane & 15) >> 2, p = lane & 3;
  const int b = blockIdx.x;
  unsigned cpf = 0;
  if (wv == 7) {
    unsigned long long pc; int off; unsigned len;
    asm volatile("s_getpc_b64 %0\nk0_code_here:\n\ts_mov_b32 %1, k1_code_begin-k0_code_here\n\ts_mov_b32 %2, k1_code_tail-k1_code_begin" : "=s"(pc), "=s"(off), "=s"(len));
    const unsigned per = (len + 32u * 128u - 1u) / (32u * 128u);
    const unsigned l = (unsigned)(b >> 3) * per + (unsigned)lane;
    if ((unsigned)lane < per && l * 128u < len) asm volatile("global_load_dword %0, %1, off" : "+v"(cpf) : "v"(pc + (long long)off + l * 128u) : "memory");
  }
  if (b == 0 && tid < NCNT) *(unsigned*)(ws + WS_CNT + (size_t)tid * 256) = 0u;
  const int ksi = b >> 2, gw = b & 3;
  const int tok = 32 * ksi + (wv < 4 ? 4 * gw + wv : 16 + 4 * gw + wv - 4);
  const u16* xr = x + (size_t)tok * D_;
  float v[9];
#pragma unroll
  for (int i = 0; i < 9; ++i) v[i] = bf2f(xr[lane + 64 * i]);
  ln9(v);
  asm volatile("" : "+v"(cpf), "+v"(v[0]));
#pragma unroll
  for (int i = 0; i < 9; ++i) *(__bf16*)(sm + wv * RS + (lane + 64 * i) * 2) = (__bf16)v[i];
  ln9(v);
#pragma unroll
  for (int i = 0; i < 9; ++i) *(__bf16*)(sm + 8 * RS + wv * RS + (lane + 64 * i) * 2) = (__bf16)v[i];
  __syncthreads();
  const rsrc_t rh = mkrsrc(ws + WS_H, 2359296), rx = mkrsrc(ws + WS_X, 2359296);
  {
    const LAS char* hs = (const LAS char*)sm + 8 * RS;
    for (int dt0 = 4 * wv; dt0 < 36; dt0 += 32) {
      const int dt = dt0 + (lane >> 4);
      const bf16x4 lo = trread(hs + q * RS + (16 * dt + 4 * p) * 2), hi = trread(hs + (4 + q) * RS + (16 * dt + 4 * p) * 2);
      st16(rh, ((ksi * 36 + dt) * 64 + gw * 16 + (lane & 15)) * 16, cat8(lo, hi));
    }
  }
  for (int i = tid; i < 576; i += 512) {
    const int rr = i & 7, ch = i >> 3;
    const int kst = ch >> 2, g = ch & 3;
    const int t = 32 * ksi + (rr < 4 ? 4 * gw + rr : 16 + 4 * gw + rr - 4);
    const u32x2 lo = *(const u32x2*)(sm + rr * RS + (32 * kst + 4 * g) * 2), hi = *(const u32x2*)(sm + rr * RS + (32 * kst + 16 + 4 * g) * 2);
    const int rb = t >> 7, st = (t >> 4) & 7, c = t & 15;
    st16(rx, (((rb * 18 + kst) * 8 + st) * 64 + g * 16 + c) * 16, (u32x4){lo[0], lo[1], hi[0], hi[1]});
  }
}

struct FragG { bf16x8 a[9]; bf16x8 w[2]; };
struct FragQ { bf16x8 a[2]; bf16x8 w[9]; };
struct PtrG { const LAS char *pa, *pw0, *pw1; };
struct OffG { unsigned a, w0, w1; };
__device__ __forceinline__ OffG offG(int w, int lane) {
  const int g = lane >> 4, q = (lane & 15) >> 2, p = lane & 3, x = 4 * (g & 1) + q;
  OffG o;
  o.a = lane * 16;
  o.w0 = 18432 + (4 * g + q) * 256 + (((2 * w) ^ x) * 32) + (p >> 1) * 16 + (p & 1) * 8;
  o.w1 = 18432 + (4 * g + q) * 256 + (((2 * w + 1) ^ x) * 32) + (p >> 1) * 16 + (p & 1) * 8;
  return o;
}
__device__ __forceinline__ PtrG ptrG(const LAS char* lds, int slot, const OffG& o) { const LAS char* s = lds + slot * SLOT; return PtrG{s + o.a, s + o.w0, s + o.w1}; }
__device__ __forceinline__ void ldG(FragG& f, const PtrG& P) {
  f.w[0] = cat8(trread(P.pw0), trread(P.pw0 + 4096)); f.w[1] = cat8(trread(P.pw1), trread(P.pw1 + 4096));
#pragma unroll
  for (int a = 0; a < 9; ++a) f.a[a] = ldsb(P.pa + a * 1024);
}
template <bool WROW, int RD, bool LOAD>
__device__ __forceinline__ void stepG(f32x4 (&acc)[2][9], const FragG& c, FragG& n, const PtrG& P, int ks) {
  bf16x4 lo0, hi0, lo1, hi1;
  auto rd = [&](int r) {
    if (!LOAD || r > 12) return;
    if (r == 0) lo0 = trread(P.pw0 + ks * 8192);
    else if (r == 1) { hi0 = trread(P.pw0 + ks * 8192 + 4096); n.w[0] = cat8(lo0, hi0); }
    else if (r == 2) n.a[0] = ldsb(P.pa + ks * 9216);
    else if (r == 3) lo1 = trread(P.pw1 + ks * 8192);
    else if (r == 4) { hi1 = trread(P.pw1 + ks * 8192 + 4096); n.w[1] = cat8(lo1, hi1); }
    else n.a[r - 4] = ldsb(P.pa + ks * 9216 + (r - 4) * 1024);
  };
#pragma unroll
  for (int i = 0; i < 18; ++i) {
    const int a = i >> 1, w = i & 1;
    if (WROW) MFMA_IP(acc[w][a], c.w[w], c.a[a]); else MFMA_IP(acc[w][a], c.a[a], c.w[w]);
    if (RD == 0) rd(i);
    else { if (i < 3) { rd(2 * i); rd(2 * i + 1); } else rd(i + 3); }
    SB();
  }
}
template <bool WROW>
__device__ __forceinline__ void runG(f32x4 (&acc)[2][9], FragG& f0, int& slot, const LAS char* lds, const OffG& o, int n) {
  PtrG P = ptrG(lds, slot, o);
#pragma unroll 1
  for (int t = 0; t < n; ++t) {
    FragG f1;
    stepG<WROW, 1, true>(acc, f0, f1, P, 1);
    WAIT_LGKM0();
    slot = slot + 1 == NS ? 0 : slot + 1;
    P = ptrG(lds, slot, o);
    BAR();
    SB();
    stepG<WROW, 0, true>(acc, f1, f0, P, 0);
  }
}
__device__ __forceinline__ void acc_settle(f32x4 (&acc)[2][9]) {
  asm volatile("s_nop 7\n\ts_nop 7" ::: "memory");
#pragma unroll
  for (int n = 0; n < 2; ++n)
#pragma unroll
    for (int a = 0; a < 9; ++a) asm volatile("" : "+v"(acc[n][a]));
}
__device__ __forceinline__ void acc_zero(f32x4 (&acc)[2][9]) {
#pragma unroll
  for (int n = 0; n < 2; ++n)
#pragma unroll
    for (int a = 0; a < 9; ++a) { acc[n][a] = (f32x4){0.f, 0.f, 0.f, 0.f}; asm volatile("" : "+v"(acc[n][a])); }
  asm volatile("s_nop 1");
}
template <bool WROW, bool LOAD>
__device__ __forceinline__ void stepQ(f32x4 (&acc)[2][9], const FragQ& c, FragQ& n, const LAS char* pa, const LAS char* pw, int ks) {
  bf16x4 lo[9];
  auto rd = [&](int r) {
    if (r == 0) n.a[0] = ldsb(pa + ks * 8192);
    else if (r == 3) n.a[1] = ldsb(pa + ks * 8192 + 1024);
    else { const int e = r < 3 ? r - 1 : r - 2, k = e >> 1;
      if (!(e & 1)) lo[k] = trread(pw + ks * 9216 + k * 32); else n.w[k] = cat8(lo[k], trread(pw + ks * 9216 + 4608 + k * 32)); }
  };
#pragma unroll
  for (int i = 0; i < 18; ++i) {
    const int k = i >> 1, s = i & 1;
    if (WROW) MFMA_IP(acc[s][k], c.w[k], c.a[s]); else MFMA_IP(acc[s][k], c.a[s], c.w[k]);
    if (LOAD) { if (i < 8) { rd(2 * i); rd(2 * i + 1); } else if (i < 12) rd(i + 8); }
    SB();
  }
}
__device__ __forceinline__ void ldQ(FragQ& f, const LAS char* pa, const LAS char* pw) {
#pragma unroll
  for (int n = 0; n < 2; ++n) f.a[n] = ldsb(pa + n * 1024);
#pragma unroll
  for (int i = 0; i < 9; ++i) f.w[i] = cat8(trread(pw + i * 32), trread(pw + 4608 + i * 32));
}
__device__ __forceinline__ unsigned offWQ(int lane) { return 16384 + (4 * (lane >> 4) + ((lane & 15) >> 2)) * 288 + 8 * (lane & 3); }
template <bool WROW>
__device__ __forceinline__ void runQ(f32x4 (&acc)[2][9], FragQ& f0, int& slot, const LAS char* lds, unsigned offA, unsigned offW, int n) {
  const LAS char* s = lds + slot * SLOT;
#pragma unroll 1
  for (int t = 0; t < n; ++t) {
    FragQ f1;
    stepQ<WROW, true>(acc, f0, f1, s + offA, s + offW, 1);
    WAIT_LGKM0();
    slot = slot + 1 == NS ? 0 : slot + 1;
    s = lds + slot * SLOT;
    BAR();
    SB();
    stepQ<WROW, true>(acc, f1, f0, s + offA, s + offW, 0);
  }
}
template <bool WROW>
__device__ __forceinline__ void lastQ(f32x4 (&acc)[2][9], FragQ& f0, int slot, const LAS char* lds, unsigned offA, unsigned offW) {
  const LAS char* s = lds + slot * SLOT;
  FragQ f1;
  stepQ<WROW, true>(acc, f0, f1, s + offA, s + offW, 1);
  WAIT_LGKM0();
  SB();
  stepQ<WROW, false>(acc, f1, f0, s + offA, s + offW, 0);
}

template <int NA>
struct LoadG {
  unsigned va[NA], vw[4]; unsigned la, lw; rsrc_t ra, rw; unsigned astep, wstep;
  __device__ __forceinline__ void init(int l, int lane, unsigned ldsbase, rsrc_t ra_, rsrc_t rw_, unsigned ldw, unsigned astep_, unsigned wstep_) {
    ra = ra_; rw = rw_; astep = astep_; wstep = wstep_;
#pragma unroll
    for (int i = 0; i < NA; ++i) { const int a = i < 4 ? l + 4 * i : 16 + l; va[i] = (a / 9) * 36864 + (a % 9) * 1024 + lane * 16; }
#pragma unroll
    for (int i = 0; i < 4; ++i) { const int row = 4 * (l + 4 * i) + (lane >> 4), cp = lane & 15; vw[i] = row * ldw + ((cp ^ ((row & 7) << 1)) * 16); }
    la = ldsbase + l * 1024; lw = ldsbase + 18432 + l * 1024;
  }
  __device__ __forceinline__ void issueW(int slot, int kb) {
    const unsigned sw = (unsigned)kb * wstep, so = slot * SLOT;
#pragma unroll
    for (int i = 0; i < 4; ++i) dma<false>(lw + so + i * 4096, vw[i], rw, sw);
  }
  template <bool SC1>
  __device__ __forceinline__ void issueA(int slot, int kb) {
    const unsigned sa = (unsigned)kb * astep, so = slot * SLOT;
#pragma unroll
    for (int i = 0; i < NA; ++i) dma<SC1>(la + so + i * 4096, va[i], ra, sa);
  }
};
template <int NW>
struct LoadQ {
  unsigned vo, vw[NW], la, lw;
  __device__ __forceinline__ void init(int l, int lane, unsigned ldsbase, unsigned ld) {
    vo = l * 1024 + lane * 16; la = ldsbase + l * 1024; lw = ldsbase + 16384 + l * 1024;
#pragma unroll
    for (int i = 0; i < NW; ++i) { const int pc = i < 4 ? l + 4 * i : 16 + l, idx = pc * 64 + lane; vw[i] = (idx / 18) * ld + (idx % 18) * 16; }
  }
  __device__ __forceinline__ void issue(int slot, rsrc_t ra, unsigned sa, rsrc_t rw, unsigned sw) {
    const unsigned so = slot * SLOT;
#pragma unroll
    for (int i = 0; i < 4; ++i) dma<false>(la + so + i * 4096, vo, ra, sa + i * 4096);
#pragma unroll
    for (int i = 0; i < NW; ++i) dma<false>(lw + so + i * 4096, vw[i], rw, sw);
  }
};

struct Args {
  const u16 *x, *wqkv, *wo, *w1, *b1, *w2, *b2;
  u16* out;
  char* ws;
};
constexpr int KWIN = 4;

__device__ __forceinline__ int kb2(int t, int rot) { const int k = t + rot; return k >= 18 ? k - 18 : k; }
template <int NA>
__device__ __forceinline__ void p1_load_g(const Args& A, int l, int lane, unsigned ldsbase, int ft, int dtile, int rot) {
  LoadG<NA> L;
  L.init(l, lane, ldsbase, mkrsrc(A.ws + WS_H + dtile * 9216, 2359296 - dtile * 9216), mkrsrc(A.w1 + ft * 128, (unsigned)(S_ * F_ * 2 - ft * 256)), F_ * 2, 73728, 64 * F_ * 2);
  constexpr int NB = 32, N = NA + 4;
  auto kbof = [&](int t) { return (t & ~(KWIN - 1)) | ((t + rot) & (KWIN - 1)); };
  auto iss = [&](int slot, int t) { const int kb = kbof(t); L.issueW(slot, kb); L.template issueA<false>(slot, kb); };
  iss(0, 0); iss(1, 1);
  WAIT_VM(N); BAR();
  iss(2, 2); iss(3, 3);
  int slot = 0;
#pragma unroll 1
  for (int t = 1; t < NB - (NS - 1); ++t) {
    WAIT_VM(N * (NS - 2));
    BAR();
    iss(slot, t + NS - 1);
    slot = slot + 1 == NS ? 0 : slot + 1;
  }
  WAIT_VM(N * 2); BAR();
  WAIT_VM(N * 1); BAR();
  WAIT_VM(0); BAR();
}
template <int NW>
__device__ __forceinline__ void p1_load_q(const Args& A, int l, int lane, unsigned ldsbase, int rb, int cg0, int cg1, int rot) {
  LoadQ<NW> L;
  L.init(l, lane, ldsbase, 3456);
  const rsrc_t ra = mkrsrc(A.ws + WS_X, 2359296), rw = mkrsrc(A.wqkv, D_ * 1728 * 2);
  constexpr int NB = 18, N = NW + 4;
  const unsigned co0 = __builtin_amdgcn_readfirstlane(cg0 * 288), co1 = __builtin_amdgcn_readfirstlane(cg1 * 288);
  auto iss = [&](int slot, int t) {
    const int seg = t >= 9; int kb = (seg ? t - 9 : t) + rot; kb = kb >= 9 ? kb - 9 : kb;
    L.issue(slot, ra, (unsigned)((rb * 9 + kb) * 16384), rw, (unsigned)kb * (64u * 3456u) + (seg ? co1 : co0));
  };
  iss(0, 0); iss(1, 1);
  WAIT_VM(N); BAR();
  iss(2, 2); iss(3, 3);
  int slot = 0;
#pragma unroll 1
  for (int t = 1; t < NB - (NS - 1); ++t) {
    WAIT_VM(N * (NS - 2));
    BAR();
    iss(slot, t + NS - 1);
    slot = slot + 1 == NS ? 0 : slot + 1;
  }
  WAIT_VM(N * 2); BAR();
  WAIT_VM(N * 1); BAR();
  WAIT_VM(0); BAR();
}
__device__ __forceinline__ void o_tile(f32x4 (&o)[2], bool two, const bf16x8& k0, const bf16x8& z0, const bf16x8 (&p0)[2], const bf16x8& k1, const bf16x8& z1, const bf16x8 (&p1)[2]) {
#pragma unroll
  for (int n = 0; n < 2; ++n) {
    f32x4 num = MFMA(k1, p1[n], ((f32x4){0.f, 0.f, 0.f, 0.f})), den = MFMA(z1, p1[n], ((f32x4){0.f, 0.f, 0.f, 0.f}));
    if (two) { num = MFMA(k0, p0[n], num); den = MFMA(z0, p0[n], den); }
#pragma unroll
    for (int r = 0; r < 4; ++r) o[n][r] = num[r] * __builtin_amdgcn_rcpf(den[r] + 1e-6f);
  }
}

__device__ __forceinline__ void ld16_asm(bf16x8& d, rsrc_t r, unsigned voff, unsigned soff) { asm volatile("buffer_load_dwordx4 %0, %1, %2, %3 offen sc1" : "=v"(d) : "v"(voff), "s"(r), "s"(soff) : "memory"); }
#define PIN(x) asm volatile("" : "+v"(x))
template <int NA>
__device__ __forceinline__ void p2_prefetch_w(const Args& A, int l, int lane, unsigned ldsbase, int st, int j, int rot) {
  LoadG<NA> L;
  L.init(l, lane, ldsbase, mkrsrc(A.ws, 0), mkrsrc(A.w2 + (size_t)j * 1152 * S_ + st * 128, (unsigned)(1152 * S_ * 2 - st * 256)), S_ * 2, 0, 64 * S_ * 2);
#pragma unroll
  for (int t = 0; t < NS - 1; ++t) L.issueW(t, kb2(t, rot));
}
template <int NA>
__device__ __forceinline__ void p2_load(const Args& A, int l, int lane, unsigned ldsbase, int st, int dt, int j, int rot) {
  constexpr int NB = 18, N = NA + 4;
  LoadG<NA> L;
  L.init(l, lane, ldsbase, mkrsrc(A.ws + WS_U + (size_t)j * 18 * 73728 + dt * 9216, 18 * 73728 - dt * 9216),
         mkrsrc(A.w2 + (size_t)j * 1152 * S_ + st * 128, (unsigned)(1152 * S_ * 2 - st * 256)), S_ * 2, 73728, 64 * S_ * 2);
  auto kbof = [&](int t) { return kb2(t, rot); };
  L.template issueA<true>(0, kbof(0)); L.template issueA<true>(1, kbof(1));
  WAIT_VM(NA); BAR();
  L.template issueA<true>(2, kbof(2));
  L.issueW(3, kbof(3)); L.template issueA<true>(3, kbof(3));
  WAIT_VM(NA + N); BAR();
  L.issueW(0, kbof(4)); L.template issueA<true>(0, kbof(4));
  int slot = 1;
  auto step = [&](int t) {
    WAIT_VM(2 * N); BAR();
    L.issueW(slot, kbof(t + 3)); L.template issueA<true>(slot, kbof(t + 3));
    slot = slot + 1 == NS ? 0 : slot + 1;
  };
  step(2); step(3);
  const rsrc_t rk = mkrsrc(A.ws + WS_KVZ + (size_t)j * 34816, 34816), rpq = mkrsrc(A.ws + WS_PQ + (size_t)(j * 128 + st * 8 + 2 * l) * 5120, 2 * 5120);
  const rsrc_t rw_o = mkrsrc(A.ws + WS_O + (size_t)(j * 128 + st * 8 + 2 * l) * 5120, 2 * 5120);
  const unsigned vl = lane * 16;
  {
    const int m = dt, fa0 = m == 0 ? 0 : 8 * m - 2;
    const unsigned kzl = ldsbase + XTRA;
    const LAS char* kzp = (const LAS char*)(size_t)kzl + vl;
    bf16x8 pq[3][2], p8[2][2];
    WAIT_VM(2 * N); BAR();
#pragma unroll
    for (int e = 0; e < 2; ++e) { const int f = 2 * l + e; dma<true>(kzl + f * 1024, vl, rk, (f < 2 ? fa0 + f : 8 * m + f - 2) * 1024); }
    if (m == 0) dma<true>(kzl + (8 + l) * 1024, vl, rk, (30 + l) * 1024);
#pragma unroll
    for (int e = 0; e < 2; ++e) { const int ks = m + e - 1 < 0 ? 0 : m + e - 1;
#pragma unroll
      for (int n = 0; n < 2; ++n) ld16_asm(pq[e][n], rpq, vl, (n * 5 + ks) * 1024); }
    L.issueW(slot, kbof(7)); L.template issueA<true>(slot, kbof(7));
    slot = slot + 1 == NS ? 0 : slot + 1;
    WAIT_VM(2 * N); BAR();
#pragma unroll
    for (int n = 0; n < 2; ++n) ld16_asm(pq[2][n], rpq, vl, (n * 5 + m + 1) * 1024);
    if (m == 0) {
#pragma unroll
      for (int e = 0; e < 2; ++e)
#pragma unroll
        for (int n = 0; n < 2; ++n) ld16_asm(p8[e][n], rpq, vl, (n * 5 + 3 + e) * 1024);
    }
    L.issueW(slot, kbof(8)); L.template issueA<true>(slot, kbof(8));
    slot = slot + 1 == NS ? 0 : slot + 1;
    step(6);
    step(7);
#pragma unroll
    for (int e = 0; e < 3; ++e) { PIN(pq[e][0]); PIN(pq[e][1]); }
    f32x4 oa[2], ob[2];
    o_tile(oa, m > 0, ldsb(kzp), ldsb(kzp + 1024), pq[0], ldsb(kzp + 2048), ldsb(kzp + 3072), pq[1]);
    PIN(oa[0]); PIN(oa[1]);
    step(8);
    o_tile(ob, true, ldsb(kzp + 4096), ldsb(kzp + 5120), pq[1], ldsb(kzp + 6144), ldsb(kzp + 7168), pq[2]);
#pragma unroll
    for (int n = 0; n < 2; ++n) st16(rw_o, vl + (n * 5 + m) * 1024, pack8(oa[n], ob[n]));
    step(9);
    if (m == 0) {
#pragma unroll
      for (int e = 0; e < 2; ++e) { PIN(p8[e][0]); PIN(p8[e][1]); }
      o_tile(oa, true, ldsb(kzp + 8192), ldsb(kzp + 9216), p8[0], ldsb(kzp + 10240), ldsb(kzp + 11264), p8[1]);
#pragma unroll
      for (int n = 0; n < 2; ++n) st16(rw_o, vl + (n * 5 + 4) * 1024, pack8(oa[n], ((f32x4){0.f, 0.f, 0.f, 0.f})));
    }
  }
  step(10);
  step(11);
  WAIT_VM(2 * N);
  if (lane == 0) add_cnt(cntp(A.ws, C_OC + st * 4 + j));
  BAR();
  L.issueW(slot, kbof(15)); L.template issueA<true>(slot, kbof(15));
  slot = slot + 1 == NS ? 0 : slot + 1;
  step(13);
  step(14);
  const rsrc_t ro = mkrsrc(A.ws + WS_O + (size_t)(j * 128 + st * 8) * 5120, 8 * 5120);
  const rsrc_t rw = mkrsrc(A.wo + (size_t)(144 * j) * D_ + 144 * dt, (unsigned)((D_ - 144 * j) * D_ * 2 - 288 * dt));
  const unsigned vo = lane * 16;
#pragma unroll
  for (int e = 0; e < 2; ++e) {
    WAIT_VM(2 * N); BAR();
    const unsigned so = ldsbase + slot * SLOT;
#pragma unroll
    for (int i = 0; i < 4; ++i) { const int pc = l + 4 * i, ks = pc >> 3, s = pc & 7; dma<true>(so + pc * 1024, vo, ro, (s * 5 + 2 * e + ks) * 1024); }
#pragma unroll
    for (int i = 0; i < NA; ++i) { const int pc = i < 4 ? l + 4 * i : 16 + l, idx = pc * 64 + lane; dma<false>(so + 16384 + pc * 1024, (idx / 18) * 1152 + (idx % 18) * 16, rw, e * 64 * 1152); }
    slot = slot + 1 == NS ? 0 : slot + 1;
  }
  constexpr int N20 = NA;
  {
    WAIT_VM(2 * N); BAR();
    const unsigned so = ldsbase + slot * SLOT;
#pragma unroll
    for (int i = 0; i < 2; ++i) { const int s = l + 4 * i; dma<true>(so + s * 1024, vo, ro, (s * 5 + 4) * 1024); }
#pragma unroll
    for (int i = 0; i < NA - 2; ++i) { const int pc = i < 2 ? l + 4 * i : 8, idx = pc * 64 + lane, row = idx / 18;
      dma<false>(so + 16384 + pc * 1024, row < 16 ? (unsigned)((128 + row) * 1152 + (idx % 18) * 16) : 0x80000000u, rw, 0); }
  }
  WAIT_VM(N + N20); BAR();
  WAIT_VM(N20); BAR();
  WAIT_VM(0); BAR();
}

__global__ __launch_bounds__(512, 2) void k1_main(Args A) {
  asm volatile("k1_code_begin:");
  __shared__ __attribute__((aligned(16))) char smem[LDS_BYTES];
  const LAS char* lds = (const LAS char*)smem;
  const unsigned ldsbase = (unsigned)(size_t)lds;
  const int tid = threadIdx.x, lane = tid & 63, wv = __builtin_amdgcn_readfirstlane(tid >> 6);
  const int b = blockIdx.x, xcd = b & 7, i = b >> 3;
  const int g = lane >> 4;
  const int j2 = xcd & 3, stl = i >> 2, st = 8 * (xcd >> 2) + stl, dt = i & 3;
  const int rot2 = ((18 * stl) / 8 + (18 * dt) / 4) % 18;
  const int nG = xcd < 4 ? 20 : 16;
  bool early = false;
  if (i < nG) {
    const int ft = (xcd < 4 ? 5 * xcd : 20 + 4 * (xcd - 4)) + (i >> 2), dtile = i & 3;
    const int rot = (KWIN * dtile) / 4 + (KWIN * (i >> 2)) / (xcd < 4 ? 5 : 4);
    const int w4 = wv & 3;
    const int f0 = ft * 128 + 32 * w4 + 4 * g;
    const u32x2 bl = *(const u32x2*)(A.b1 + f0), bh = *(const u32x2*)(A.b1 + f0 + 16);
    const rsrc_t ru = mkrsrc(A.ws + WS_U + ((size_t)(ft * 4 + w4) * 36 + dtile * 9) * 1024, 9216);
    auto gelu_store = [&](int a, const f32x4& c0, const f32x4& c1) {
      st16(ru, a * 1024 + lane * 16, (u32x4){gelu_pk((f32x2){c0[0], c0[1]}, bl[0]), gelu_pk((f32x2){c0[2], c0[3]}, bl[1]), gelu_pk((f32x2){c1[0], c1[1]}, bh[0]), gelu_pk((f32x2){c1[2], c1[3]}, bh[1])});
    };
    if (wv >= 4) {
      if (wv < 6) { p1_load_g<5>(A, wv - 4, lane, ldsbase, ft, dtile, rot); p2_prefetch_w<5>(A, wv - 4, lane, ldsbase, st, j2, rot2); }
      else { p1_load_g<4>(A, wv - 4, lane, ldsbase, ft, dtile, rot); p2_prefetch_w<4>(A, wv - 4, lane, ldsbase, st, j2, rot2); }
    } else {
      f32x4 acc[2][9];
      acc_zero(acc);
      const OffG o = offG(wv, lane);
      FragG fa; int slot = 0;
      BAR();
      ldG(fa, ptrG(lds, 0, o));
      SB();
      runG<true>(acc, fa, slot, lds, o, 31);
      { FragG fb; const PtrG P = ptrG(lds, slot, o);
        stepG<true, 1, true>(acc, fa, fb, P, 1); WAIT_LGKM0(); SB(); stepG<true, 0, false>(acc, fb, fa, P, 0); }
      acc_settle(acc);
#pragma unroll
      for (int a = 0; a < 9; ++a) gelu_store(a, acc[0][a], acc[1][a]);
    }
    WAIT_VM(0);
    BAR();
    if (tid == 0) st_flag(cntp(A.ws, C_UF + ft * 4 + dtile));
  } else {
    const int u = i - nG, pr = xcd & 1, nkv = xcd < 4 ? 6 : 10;
    const unsigned offA = 2 * (wv & 3) * 1024 + lane * 16, offW = offWQ(lane);
    if (u >= nkv + 4) {
      const int sp = xcd * 2 + (u - nkv - 4);
      if (wv >= 4) { if (wv < 6) p2_prefetch_w<5>(A, wv - 4, lane, ldsbase, st, j2, rot2); else p2_prefetch_w<4>(A, wv - 4, lane, ldsbase, st, j2, rot2); }
      const int j = sp & 3, part = sp >> 2;
      if (wv == 0) wait_words(cntp(A.ws, C_KVC + j), 16u);
      BAR();
      float* tot = (float*)(smem + XTRA);
      {
        const int h0 = part == 0 ? 0 : (part == 1 ? 1 : 4), c0 = (h0 * 324) >> 3;
        const int ch = tid < 164 ? c0 + tid : 324 + (tid - 164);
        if (tid < 182) {
          const rsrc_t rp = mkrsrc(A.ws + WS_KVP, 64 * 5472);
          u32x4 v[16];
#pragma unroll
          for (int r = 0; r < 16; ++r) v[r] = ld16(rp, (unsigned)((r * 4 + j) * 5472 + ch * 16));
          f32x4 lo = {0.f, 0.f, 0.f, 0.f}, hi = {0.f, 0.f, 0.f, 0.f};
#pragma unroll
          for (int r = 0; r < 16; ++r) { lo += unpk_lo(v[r]); hi += unpk_hi(v[r]); }
          *(f32x4*)(tot + ch * 8) = lo; *(f32x4*)(tot + ch * 8 + 4) = hi;
        }
      }
      __syncthreads();
      const rsrc_t rz = mkrsrc(A.ws + WS_KVZ + (size_t)j * 34816, 34816);
#pragma unroll
      for (int k = 0; k < 2; ++k) {
        const int fi = part * 9 + (k ? 8 : wv);
        if (fi < 34 && (k == 0 || wv == 0)) {
          const int pi = fi >> 1, kind = fi & 1;
          const int ii = (pi + 1) >> 1, m = (ii == 0 ? 0 : (ii - 1) >> 1) + (pi > 0 && !(pi & 1) ? 1 : 0);
          const int cp = 16 * ii + (lane & 15), hp = (cp * 3641) >> 16;
          const int c0 = 32 * m + 4 * g, mul = kind ? 1 : 18;
          const int base = kind ? 2592 + c0 : 18 * (c0 - hp) + cp, rel = c0 - 18 * hp;
          float val[8];
#pragma unroll
          for (int jj = 0; jj < 8; ++jj) {
            const int o = jj < 4 ? jj : 12 + jj;
            const bool ok = (unsigned)(rel + o) < 18u;
            const float v = tot[ok ? base + o * mul : 0];
            val[jj] = ok ? v : 0.f;
          }
          st16(rz, fi * 1024 + lane * 16, (u32x4){pack2(val[0], val[1]), pack2(val[2], val[3]), pack2(val[4], val[5]), pack2(val[6], val[7])});
        }
      }
      GAS unsigned* wpe = cntp(A.ws, C_UF + (9 * j2 + (kb2(lane < 2 ? lane : 0, rot2) >> 1)) * 4 + dt);
      unsigned pve = 0;
      if (wv == 4) poll_issue(pve, wpe);
      WAIT_VM(0);
      BAR();
      if (tid == 0) add_cnt(cntp(A.ws, C_KF + j));
      if (wv == 4) poll_check(pve, wpe, 1u);
    } else if (u >= nkv) {
      const int rb = 4 * (xcd >> 1) + (u - nkv), j0 = 2 * pr;
      const int rot = (9 * (u - nkv)) / 4 + 1;
      if (wv >= 4) {
        if (wv < 6) p1_load_q<5>(A, wv - 4, lane, ldsbase, rb, j0, j0 + 1, rot); else p1_load_q<4>(A, wv - 4, lane, ldsbase, rb, j0, j0 + 1, rot);
      } else {
        FragQ f0; f32x4 acc[2][9]; int slot = 0;
        auto epi = [&](int j) {
#pragma unroll
          for (int n = 0; n < 2; ++n) {
            const rsrc_t P = mkrsrc(A.ws + WS_PQ + ((size_t)(j * 128 + rb * 8 + 2 * wv + n) * 5) * 1024, 5120);
#pragma unroll
            for (int m = 0; m < 5; ++m) {
              const f32x4 p0 = phi4(acc[n][2 * m]), p1 = m < 4 ? phi4(acc[n][m < 4 ? 2 * m + 1 : 0]) : (f32x4){0.f, 0.f, 0.f, 0.f};
              st16(P, m * 1024 + lane * 16, pack8(p0, p1));
            }
          }
        };
        BAR();
        ldQ(f0, lds + offA, lds + offW);
        acc_zero(acc);
        SB();
        runQ<true>(acc, f0, slot, lds, offA, offW, 9);
        acc_settle(acc);
        epi(j0);
        acc_zero(acc);
        SB();
        runQ<true>(acc, f0, slot, lds, offA, offW, 8);
        lastQ<true>(acc, f0, slot, lds, offA, offW);
        acc_settle(acc);
        epi(j0 + 1);
      }
      WAIT_VM(0);
      BAR();
      if (tid == 0) st_flag(cntp(A.ws, C_PQF + rb * 2 + pr));
    } else {
      const int rb = (xcd < 4 ? 3 * (xcd >> 1) : 6 + 5 * ((xcd - 4) >> 1)) + (u >> 1), j = 2 * pr + (u & 1);
      const int rot = ((9 * (u >> 1)) / (nkv >> 1) + 3 * (u & 1)) % 9;
      LAS char* fr = (LAS char*)smem;
      float* cmp = (float*)(smem + XTRA);
      if (wv >= 4) {
        if (wv < 6) p1_load_q<5>(A, wv - 4, lane, ldsbase, rb, 4 + j, 8 + j, rot); else p1_load_q<4>(A, wv - 4, lane, ldsbase, rb, 4 + j, 8 + j, rot);
        BAR();
      } else {
        bf16x8 pk[9];
        FragQ f0; f32x4 acc[2][9]; int slot = 0;
        BAR();
        ldQ(f0, lds + offA, lds + offW);
        acc_zero(acc);
        SB();
        runQ<false>(acc, f0, slot, lds, offA, offW, 9);
        acc_settle(acc);
#pragma unroll
        for (int c = 0; c < 9; ++c) pk[c] = pack8(phi4(acc[0][c]), phi4(acc[1][c]));
        acc_zero(acc);
        SB();
        runQ<false>(acc, f0, slot, lds, offA, offW, 8);
        lastQ<false>(acc, f0, slot, lds, offA, offW);
        acc_settle(acc);
        BAR();
#pragma unroll
        for (int c = 0; c < 9; ++c) {
          *(LAS bf16x8*)(fr + (c * 4 + wv) * 1024 + lane * 16) = pk[c];
          *(LAS bf16x8*)(fr + ((9 + c) * 4 + wv) * 1024 + lane * 16) = pack8(acc[0][c], acc[1][c]);
        }
        if (wv == 0) *(LAS u32x4*)(fr + 73728 + lane * 16) = (u32x4){0x3f803f80u, 0x3f803f80u, 0x3f803f80u, 0x3f803f80u};
      }
      __syncthreads();
      {
        const int cl = lane & 15;
        f32x4 d[5];
        int ja[5], jc[5];
#pragma unroll
        for (int k = 0; k < 5; ++k) {
          const int jb0 = wv + 8 * k, jb = jb0 < 34 ? jb0 : 33;
          const int isz = jb >= 25, t = jb < 2 ? 0 : jb - 2, t3 = (t * 86) >> 8;
          ja[k] = isz ? jb - 25 : (jb < 2 ? 0 : 1 + t3);
          jc[k] = isz ? -1 : (jb < 2 ? jb : t3 + t - 3 * t3);
        }
        bf16x8 fa[2][4], fb[2][4];
        auto ldjob = [&](int k, int buf) {
          const LAS char* pa = fr + ja[k] * 4096 + lane * 16;
          const LAS char* pb = fr + (jc[k] < 0 ? 73728 : (9 + jc[k]) * 4096) + lane * 16;
          const int sb = jc[k] < 0 ? 0 : 1024;
#pragma unroll
          for (int r = 0; r < 4; ++r) { fa[buf][r] = ldsb(pa + r * 1024); fb[buf][r] = ldsb(pb + r * sb); }
        };
        ldjob(0, 0);
#pragma unroll
        for (int k = 0; k < 5; ++k) {
          if (k < 4) ldjob(k + 1, (k + 1) & 1);
          d[k] = (f32x4){0.f, 0.f, 0.f, 0.f};
#pragma unroll
          for (int r = 0; r < 4; ++r) d[k] = MFMA(fa[k & 1][r], fb[k & 1][r], d[k]);
        }
        LAS float* cm = (LAS float*)(smem + XTRA);
#pragma unroll
        for (int k = 0; k < 5; ++k) {
          const bool isz = jc[k] < 0;
          const int row0 = 16 * ja[k] + 4 * g, col = 16 * jc[k] + cl, hc = (col * 3641) >> 16;
          const int lo = isz ? (cl == 0 ? row0 - 8 : 4096) : 18 * hc;
          const int idx = isz ? 2592 + row0 : 18 * (row0 - hc) + col, step = isz ? 1 : 18;
#pragma unroll
          for (int r = 0; r < 4; ++r) cm[(unsigned)(row0 + r - lo) < 18u ? idx + r * step : 2736 + lane] = d[k][r];
        }
      }
      __syncthreads();
      const rsrc_t rp = mkrsrc(A.ws + WS_KVP, 64 * 5472);
      if (tid < 342) st16(rp, (unsigned)((rb * 4 + j) * 5472 + tid * 16), pack8(*(const f32x4*)(cmp + tid * 8), *(const f32x4*)(cmp + tid * 8 + 4)));
      WAIT_VM(0);
      BAR();
      if (tid == 0) add_cnt(cntp(A.ws, C_KVC + j));
    }
    early = u >= nkv + 4;
    __syncthreads();
    if (wv >= 4 && u < nkv + 4) { if (wv < 6) p2_prefetch_w<5>(A, wv - 4, lane, ldsbase, st, j2, rot2); else p2_prefetch_w<4>(A, wv - 4, lane, ldsbase, st, j2, rot2); }
  }
  const int T = st * 4 + dt;
  const rsrc_t rx = mkrsrc(A.ws + WS_XCH, 64u * 16u * 9216u);
  LAS f32x4* own = (LAS f32x4*)(smem + SLOT);
  const int na = wv == 0 ? 2 : 1;
  u32x2 xr[2][2]; float b2v[2];
  auto ld_resid = [&]() {
#pragma unroll
    for (int n = 0; n < 2; ++n) {
      const int s = st * 128 + 32 * j2 + 16 * n + (lane & 15);
      b2v[n] = bf2f(A.b2[s]);
#pragma unroll
      for (int k = 0; k < 2; ++k) xr[k][n] = *(const u32x2*)(A.x + (size_t)s * D_ + dt * 144 + 16 * (k ? 8 : wv) + 4 * g);
    }
  };
  if (!early) {
    if (wv == 4) { const int k = kb2(lane < 2 ? lane : 0, rot2); wait_words(cntp(A.ws, C_UF + (9 * j2 + (k >> 1)) * 4 + dt), 1u); }
    BAR();
  }
  if (wv >= 4) {
    if (wv < 6) p2_load<5>(A, wv - 4, lane, ldsbase, st, dt, j2, rot2); else p2_load<4>(A, wv - 4, lane, ldsbase, st, dt, j2, rot2);
    ld_resid();
  } else {
    f32x4 acc[2][9];
    acc_zero(acc);
    const OffG o = offG(wv, lane);
    FragG fa; int slot = 0;
    if (wv == 0) { const int k = kb2(2 + (lane < 3 ? lane : 0), rot2); wait_words(cntp(A.ws, C_UF + (9 * j2 + (k >> 1)) * 4 + dt), 1u); }
    BAR();
    ldG(fa, ptrG(lds, 0, o));
    SB();
    GAS unsigned* wp0 = cntp(A.ws, C_UF + (9 * j2 + (lane < 9 ? lane : 0)) * 4 + dt);
    unsigned pv0 = 0;
    if (wv == 0) poll_issue(pv0, wp0);
    runG<false>(acc, fa, slot, lds, o, 1);
    if (wv == 0) poll_check(pv0, wp0, 1u);
    GAS unsigned* wp1 = lane == 0 ? cntp(A.ws, C_KF + j2) : cntp(A.ws, C_PQF + st * 2 + (j2 >> 1));
    runG<false>(acc, fa, slot, lds, o, 1);
    unsigned pv1 = 0;
    if (wv == 0) poll_issue(pv1, wp1);
    runG<false>(acc, fa, slot, lds, o, 1);
    if (wv == 0) poll_check(pv1, wp1, lane == 0 ? 4u : 1u);
    GAS unsigned* wp2 = cntp(A.ws, C_OC + st * 4 + j2);
    runG<false>(acc, fa, slot, lds, o, 10);
    unsigned pv2 = 0;
    if (wv == 0) poll_issue(pv2, wp2);
    runG<false>(acc, fa, slot, lds, o, 1);
    if (wv == 0) poll_check(pv2, wp2, 16u);
    runG<false>(acc, fa, slot, lds, o, 3);
    ld_resid();
    const unsigned offA = 2 * wv * 1024 + lane * 16, offW = offWQ(lane);
    FragQ q0;
    { FragG fb; const PtrG P = ptrG(lds, slot, o);
      stepG<false, 1, true>(acc, fa, fb, P, 1);
      WAIT_LGKM0();
      slot = slot + 1 == NS ? 0 : slot + 1;
      BAR();
      ldQ(q0, lds + slot * SLOT + offA, lds + slot * SLOT + offW);
      SB();
      stepG<false, 0, false>(acc, fb, fa, P, 0); }
    runQ<true>(acc, q0, slot, lds, offA, offW, 2);
    { FragQ q1; stepQ<true, false>(acc, q0, q1, lds, lds, 0); }
    acc_settle(acc);
    if (wv != j2) {
      const unsigned base = (unsigned)(((T * 4 + wv) * 4 + j2) * 9216) + lane * 16;
#pragma unroll
      for (int a = 0; a < 9; ++a) st16(rx, base + a * 1024, pack8(acc[0][a], acc[1][a]));
      WAIT_VM(0);
      if (lane == 0) add_cnt(cntp(A.ws, C_XC + T * 4 + wv));
    } else {
#pragma unroll
      for (int n = 0; n < 2; ++n)
#pragma unroll
        for (int a = 0; a < 9; ++a) own[(n * 9 + a) * 64 + lane] = acc[n][a];
      WAIT_LGKM0();
    }
  }
  if (wv == 4) wait_words(cntp(A.ws, C_XC + T * 4 + j2), 3u);
  BAR();
  u32x4 pv[2][3];
#pragma unroll
  for (int k = 0; k < 2; ++k)
#pragma unroll
    for (int e = 0; e < 3; ++e) { const int src = e + (e >= j2 ? 1 : 0);
      pv[k][e] = ld16(rx, (k < na) ? (unsigned)(((T * 4 + j2) * 4 + src) * 9216 + (k ? 8 : wv) * 1024) + lane * 16 : 0x80000000u); }
#pragma unroll
  for (int k = 0; k < 2; ++k) {
    if (k < na) {
      const int a = k ? 8 : wv;
#pragma unroll
      for (int n = 0; n < 2; ++n) {
        f32x4 sum = own[(n * 9 + a) * 64 + lane];
#pragma unroll
        for (int e = 0; e < 3; ++e) sum += n ? unpk_hi(pv[k][e]) : unpk_lo(pv[k][e]);
        const int s = st * 128 + 32 * j2 + 16 * n + (lane & 15), d = dt * 144 + 16 * a + 4 * g;
        const unsigned y0 = xr[k][n][0], y1 = xr[k][n][1];
        const float x0 = __uint_as_float(y0 << 16), x1 = __uint_as_float(y0 & 0xffff0000u), x2 = __uint_as_float(y1 << 16), x3 = __uint_as_float(y1 & 0xffff0000u);
        *(u32x2*)(A.out + (size_t)s * D_ + d) = (u32x2){pack2(x0 + (sum[0] + b2v[n]), x1 + (sum[1] + b2v[n])), pack2(x2 + (sum[2] + b2v[n]), x3 + (sum[3] + b2v[n]))};
      }
    }
  }
  asm volatile("k1_code_tail:");
}

extern "C" void kernel_launch(void* const* d_in, const int* in_sizes, int n_in, void* d_out, int out_size, void* d_ws, size_t ws_size, hipStream_t stream) {
  Args A;
  A.x = (const u16*)d_in[0]; A.wqkv = (const u16*)d_in[1]; A.wo = (const u16*)d_in[2];
  A.w1 = (const u16*)d_in[4]; A.b1 = (const u16*)d_in[5]; A.w2 = (const u16*)d_in[6]; A.b2 = (const u16*)d_in[7];
  A.out = (u16*)d_out;
  A.ws = (char*)d_ws;
  k0_prep<<<256, 512, 0, stream>>>(A.x, A.ws);
  k1_main<<<256, 512, 0, stream>>>(A);
}
```
